```python
import torch

def _softmax(x, axis=-1):
    return torch.softmax(x, dim=axis)
def _gelu(x):
    return torch.nn.functional.gelu(x, approximate="tanh")
def _silu(x):
    return torch.nn.functional.silu(x)
def _sigmoid(x):
    return torch.sigmoid(x)
def _layernorm(x, axis=-1, eps=1e-5):
    m = x.mean(dim=axis, keepdim=True)
    v = x.var(dim=axis, keepdim=True, unbiased=False)
    return (x - m) / torch.sqrt(v + eps)
def _scatter_add(tgt, idx, src, axis=0):
    return tgt.clone().index_add_(axis, idx, src)
def _segment_sum(src, seg, n):
    out = torch.zeros((n,) + src.shape[1:], dtype=src.dtype)
    return out.index_add_(0, seg, src)
def _pad(x, pw):
    flat = tuple(v for pair in reversed(pw) for v in pair)
    return torch.nn.functional.pad(x, flat)

kernel_name = 'gated_mix__ssm_conv_s4d__dense_activation_squared_relu__prompt'

def setup_inputs():

    _w = (lambda a: (lambda t: t.to(torch.bfloat16) if t.is_floating_point() else t)(torch.as_tensor(a)))
    def _A_setup():
        Bsz, L, D, N = 1, 2048, 640, 128
        return {
            "u": _w((lambda *s: torch.randn(*s, dtype=torch.bfloat16) * 0.05)(Bsz, L, D)),
            "log_A_real": _w((lambda *s: torch.randn(*s, dtype=torch.bfloat16) * 6.4)(D, N) * 0.5),
            "A_imag": _w((lambda *s: torch.randn(*s, dtype=torch.bfloat16) * 0.1)(D, N) * 3.14),
            "C_real": _w((lambda *s: torch.randn(*s, dtype=torch.bfloat16) * 0.4)(D, N) / N ** 0.5),
            "C_imag": _w((lambda *s: torch.randn(*s, dtype=torch.bfloat16) * 0.4)(D, N) / N ** 0.5),
            "log_dt": _w((lambda *s: torch.randn(*s, dtype=torch.bfloat16) * 0.1)(D) * 0.5 - 4.0),
            "D_skip": _w((lambda *s: torch.randn(*s, dtype=torch.bfloat16) * 0.1)(D)),
        }
    _A_in = _A_setup()

    def _B_setup():
        B, S, D, F = 1, 2048, 640, 1728
        return {
            "x": _w((lambda *s: torch.randn(*s, dtype=torch.bfloat16) * 0.05)(B, S, D)),
            "w1": _w((lambda *s: torch.randn(*s, dtype=torch.bfloat16) * 0.05)(D, F)),
            "b1": _w((lambda *s: torch.randn(*s, dtype=torch.bfloat16) * 0.05)(F)),
            "w2": _w((lambda *s: torch.randn(*s, dtype=torch.bfloat16) * 0.05)(F, D)),
            "b2": _w((lambda *s: torch.randn(*s, dtype=torch.bfloat16) * 0.05)(D)),
        }
    _B_in = _B_setup()

    return {
        "A_u": _A_in["u"],
        "A_log_A_real": _A_in["log_A_real"],
        "A_A_imag": _A_in["A_imag"],
        "A_C_real": _A_in["C_real"],
        "A_C_imag": _A_in["C_imag"],
        "A_log_dt": _A_in["log_dt"],
        "A_D_skip": _A_in["D_skip"],
        "B_x": _B_in["x"],
        "B_w1": _B_in["w1"],
        "B_b1": _B_in["b1"],
        "B_w2": _B_in["w2"],
        "B_b2": _B_in["b2"],
        "gate_w": _w((lambda *s: torch.randn(*s, dtype=torch.bfloat16) * 0.05)(640) * 16.0),
    }

def reference(A_u, A_log_A_real, A_A_imag, A_C_real, A_C_imag, A_log_dt, A_D_skip, B_x, B_w1, B_b1, B_w2, B_b2, gate_w):
    def _A_ref(u, log_A_real, A_imag, C_real, C_imag, log_dt, D_skip):
        L = u.shape[1]
        dt = torch.exp(log_dt)
        A = -torch.exp(log_A_real) + 1j * A_imag
        C = (C_real + 1j * C_imag) * dt[:, None]
        pos = (lambda x, dt: x.to(dt))(torch.arange(L), log_dt.dtype)
        P = torch.exp((A * dt[:, None])[:, :, None] * pos[None, None, :])
        K = 2.0 * torch.einsum("dn,dnl->dl", C, P).real
        L2 = 2 * L
        u_t = (lambda x, *d: x.permute(*d))(u, 0, 2, 1)
        Kf = (lambda x, *a, **k: torch.fft.rfft(x.float(), *a, **k))(K, L2)
        Uf = (lambda x, *a, **k: torch.fft.rfft(x.float(), *a, **k))(u_t, L2)
        y = (lambda x, *a, **k: torch.fft.irfft(x, *a, **k).to(torch.bfloat16))(Uf * Kf[None, :, :], L2)[:, :, :L]
        y = (lambda x, *d: x.permute(*d))(y, 0, 2, 1)
        return y + u * D_skip[None, None, :]

    def _B_ref(x, w1, b1, w2, b2):
        h = torch.einsum("bsd,df->bsf", x, w1) + b1
        zero = torch.zeros_like(h)
        r = torch.where(h > zero, h, zero)
        h = r * r
        out = torch.einsum("bsf,fd->bsd", h, w2) + b2
        return out

    x = A_u
    g = _sigmoid((x * gate_w).sum(-1, keepdims=True))
    a = _A_ref(x, A_log_A_real, A_A_imag, A_C_real, A_C_imag, A_log_dt, A_D_skip)
    b = _B_ref(x, B_w1, B_b1, B_w2, B_b2)
    return g * a + (1 - g) * b
```

```cpp
#include <hip/hip_runtime.h>
#include <hip/hip_bf16.h>
typedef unsigned short u16;
__device__ __forceinline__ float bf2f(u16 x){ return __uint_as_float(((unsigned)x)<<16); }
__device__ __forceinline__ u16 f2bf(float f){ __hip_bfloat16 b = __float2bfloat16(f); return *(u16*)&b; }
__device__ __forceinline__ float rbf(float f){ return bf2f(f2bf(f)); }
constexpr int L=2048, D=640, N=128, F=1728;

__global__ void kgen(const u16* lar,const u16* aim,const u16* cr,const u16* ci,const u16* ldt,float* K){
  int d=blockIdx.x;
  __shared__ float sa[N],sb[N],scr[N],sci[N];
  float dt=rbf(expf(bf2f(ldt[d])));
  for(int n=threadIdx.x;n<N;n+=blockDim.x){
    sa[n]=-rbf(expf(bf2f(lar[d*N+n])))*dt; sb[n]=bf2f(aim[d*N+n])*dt; scr[n]=bf2f(cr[d*N+n])*dt; sci[n]=bf2f(ci[d*N+n])*dt; }
  __syncthreads();
  for(int l=threadIdx.x;l<L;l+=blockDim.x){
    float p=rbf((float)l); float s=0;
    for(int n=0;n<N;n++){ float e=expf(sa[n]*p); float sn,cs; sincosf(sb[n]*p,&sn,&cs); s+=scr[n]*e*cs-sci[n]*e*sn; }
    K[d*L+l]=2.f*s; }
}
__global__ void conv(const u16* u,const float* K,float* y){
  int d=blockIdx.x; __shared__ float su[L], sk[L];
  for(int t=threadIdx.x;t<L;t+=blockDim.x){ su[t]=bf2f(u[t*D+d]); sk[t]=K[d*L+t]; }
  __syncthreads();
  for(int t=threadIdx.x;t<L;t+=blockDim.x){ float s=0; for(int l=0;l<=t;l++) s+=sk[l]*su[t-l]; y[t*D+d]=s; }
}
__global__ void gemm1(const u16* x,const u16* w1,const u16* b1,u16* h){
  int t=blockIdx.x; __shared__ float sx[D];
  for(int i=threadIdx.x;i<D;i+=blockDim.x) sx[i]=bf2f(x[t*D+i]);
  __syncthreads();
  for(int f=threadIdx.x;f<F;f+=blockDim.x){ float s=0; for(int k=0;k<D;k++) s+=sx[k]*bf2f(w1[k*F+f]);
    float v=rbf(rbf(s)+bf2f(b1[f])); v=v>0?v:0; h[t*F+f]=f2bf(v*v); }
}
__global__ void gemm2(const u16* x,const u16* h,const u16* w2,const u16* b2,const float* y,const u16* dsk,const u16* gw,u16* out){
  int t=blockIdx.x; __shared__ float sh[F]; __shared__ float red[256];
  for(int i=threadIdx.x;i<F;i+=blockDim.x) sh[i]=bf2f(h[t*F+i]);
  float gs=0; for(int i=threadIdx.x;i<D;i+=blockDim.x) gs+=rbf(bf2f(x[t*D+i])*bf2f(gw[i]));
  red[threadIdx.x]=gs; __syncthreads();
  for(int s=128;s>0;s>>=1){ if(threadIdx.x<s) red[threadIdx.x]+=red[threadIdx.x+s]; __syncthreads(); }
  float g=rbf(1.f/(1.f+expf(-rbf(red[0]))));
  for(int d=threadIdx.x;d<D;d+=blockDim.x){ float s=0; for(int k=0;k<F;k++) s+=sh[k]*bf2f(w2[k*D+d]);
    float b=rbf(rbf(s)+bf2f(b2[d]));
    float a=rbf(rbf(y[t*D+d])+rbf(bf2f(x[t*D+d])*bf2f(dsk[d])));
    out[t*D+d]=f2bf(rbf(g*a)+rbf(rbf(1.f-g)*b)); }
}
extern "C" void kernel_launch(void* const* d_in, const int* in_sizes, int n_in, void* d_out, int out_size, void* d_ws, size_t ws_size, hipStream_t stream) {
  const u16* u=(const u16*)d_in[0];
  float* K=(float*)d_ws; float* y=K+D*L; u16* h=(u16*)(y+D*L);
  kgen<<<D,256,0,stream>>>((const u16*)d_in[1],(const u16*)d_in[2],(const u16*)d_in[3],(const u16*)d_in[4],(const u16*)d_in[5],K);
  conv<<<D,256,0,stream>>>(u,K,y);
  gemm1<<<L,256,0,stream>>>(u,(const u16*)d_in[8],(const u16*)d_in[9],h);
  gemm2<<<L,256,0,stream>>>(u,h,(const u16*)d_in[10],(const u16*)d_in[11],y,(const u16*)d_in[6],(const u16*)d_in[12],(u16*)d_out);
}
```

```cpp
#include <hip/hip_runtime.h>
#include <hip/hip_bf16.h>
typedef unsigned short u16;
__device__ __forceinline__ float bf2f(u16 x){ return __uint_as_float(((unsigned)x)<<16); }
__device__ __forceinline__ u16 f2bf(float f){ __hip_bfloat16 b = __float2bfloat16(f); return *(u16*)&b; }
__device__ __forceinline__ float rbf(float f){ return bf2f(f2bf(f)); }
constexpr int L=2048, D=640, N=128, F=1728;

__global__ void kgen(const u16* lar,const u16* aim,const u16* cr,const u16* ci,const u16* ldt,float* K){
  int d=blockIdx.x;
  __shared__ float sa[N],sb[N],scr[N],sci[N];
  float dt=rbf(expf(bf2f(ldt[d])));
  for(int n=threadIdx.x;n<N;n+=blockDim.x){
    sa[n]=-rbf(expf(bf2f(lar[d*N+n])))*dt; sb[n]=bf2f(aim[d*N+n])*dt; scr[n]=bf2f(cr[d*N+n])*dt; sci[n]=bf2f(ci[d*N+n])*dt; }
  __syncthreads();
  for(int l=threadIdx.x;l<L;l+=blockDim.x){
    float p=rbf((float)l); float s=0;
    for(int n=0;n<N;n++){ float e=expf(sa[n]*p); float sn,cs; sincosf(sb[n]*p,&sn,&cs); s+=scr[n]*e*cs-sci[n]*e*sn; }
    K[d*L+l]=2.f*s; }
}
__global__ void conv(const u16* u,const float* K,float* y){
  int d=blockIdx.x; __shared__ float su[L], sk[L];
  for(int t=threadIdx.x;t<L;t+=blockDim.x){ su[t]=bf2f(u[t*D+d]); sk[t]=K[d*L+t]; }
  __syncthreads();
  for(int t=threadIdx.x;t<L;t+=blockDim.x){ float s=0; for(int l=0;l<=t;l++) s+=sk[l]*su[t-l]; y[t*D+d]=s; }
}
__global__ void gemm1(const u16* x,const u16* w1,const u16* b1,u16* h){
  int t=blockIdx.x; __shared__ float sx[D];
  for(int i=threadIdx.x;i<D;i+=blockDim.x) sx[i]=bf2f(x[t*D+i]);
  __syncthreads();
  for(int f=threadIdx.x;f<F;f+=blockDim.x){ float s=0; for(int k=0;k<D;k++) s+=sx[k]*bf2f(w1[k*F+f]);
    float v=rbf(rbf(s)+bf2f(b1[f])); v=v>0?v:0; h[t*F+f]=f2bf(v*v); }
}
__global__ void gemm2(const u16* x,const u16* h,const u16* w2,const u16* b2,const float* y,const u16* dsk,const u16* gw,u16* out){
  int t=blockIdx.x; __shared__ float sh[F]; __shared__ float red[256];
  for(int i=threadIdx.x;i<F;i+=blockDim.x) sh[i]=bf2f(h[t*F+i]);
  float gs=0; for(int i=threadIdx.x;i<D;i+=blockDim.x) gs+=rbf(bf2f(x[t*D+i])*bf2f(gw[i]));
  red[threadIdx.x]=gs; __syncthreads();
  for(int s=128;s>0;s>>=1){ if(threadIdx.x<s) red[threadIdx.x]+=red[threadIdx.x+s]; __syncthreads(); }
  float g=rbf(1.f/(1.f+expf(-rbf(red[0]))));
  for(int d=threadIdx.x;d<D;d+=blockDim.x){ float s=0; for(int k=0;k<F;k++) s+=sh[k]*bf2f(w2[k*D+d]);
    float b=rbf(rbf(s)+bf2f(b2[d]));
    float a=rbf(rbf(y[t*D+d])+rbf(bf2f(x[t*D+d])*bf2f(dsk[d])));
    out[t*D+d]=f2bf(rbf(g*a)+rbf(rbf(1.f-g)*b)); }
}

__global__ void chase(const unsigned* p,int n,unsigned stride,unsigned zero,unsigned* sink){
  unsigned idx=0,acc=0;
  for(int i=0;i<n;i++){ unsigned v=p[idx]; acc+=v; idx+=stride+(v&zero); }
  if(acc==0x12345678u && zero) sink[0]=acc;
}
extern "C" void kernel_launch(void* const* d_in, const int* in_sizes, int n_in, void* d_out, int out_size, void* d_ws, size_t ws_size, hipStream_t stream) {
  const u16* u=(const u16*)d_in[0];
  float* K=(float*)d_ws; float* y=K+D*L; u16* h=(u16*)(y+D*L);
  chase<<<1,1,0,stream>>>((const unsigned*)d_in[7],1,2048+32,0,(unsigned*)d_ws+(60<<20));
  kgen<<<D,256,0,stream>>>((const u16*)d_in[1],(const u16*)d_in[2],(const u16*)d_in[3],(const u16*)d_in[4],(const u16*)d_in[5],K);
  conv<<<D,256,0,stream>>>(u,K,y);
  gemm1<<<L,256,0,stream>>>(u,(const u16*)d_in[8],(const u16*)d_in[9],h);
  gemm2<<<L,256,0,stream>>>(u,h,(const u16*)d_in[10],(const u16*)d_in[11],y,(const u16*)d_in[6],(const u16*)d_in[12],(u16*)d_out);
}
```

```cpp
#include <hip/hip_runtime.h>
#include <hip/hip_bf16.h>

typedef unsigned short u16;
typedef unsigned int u32;
typedef unsigned long long u64;
typedef short s16x4 __attribute__((ext_vector_type(4)));
typedef __bf16 bf16x2 __attribute__((ext_vector_type(2)));
typedef __bf16 bf16x8 __attribute__((ext_vector_type(8)));
typedef _Float16 f16x2 __attribute__((ext_vector_type(2)));
typedef _Float16 f16x8 __attribute__((ext_vector_type(8)));
typedef float f32x2 __attribute__((ext_vector_type(2)));
typedef float f32x4 __attribute__((ext_vector_type(4)));
typedef u32 u32x2 __attribute__((ext_vector_type(2)));
typedef int i32x4 __attribute__((ext_vector_type(4)));
typedef u32 u32x4 __attribute__((ext_vector_type(4)));
typedef __amdgpu_buffer_rsrc_t rsrc_t;
#define LAS __attribute__((address_space(3)))
#define GAS __attribute__((address_space(1)))
#define RLX_AGENT __ATOMIC_RELAXED, __HIP_MEMORY_SCOPE_AGENT
#define RLX_WG __ATOMIC_RELAXED, __HIP_MEMORY_SCOPE_WORKGROUP
#define DEV __device__ __forceinline__
#define HSLEEP 24
#define G2PRIO 1
#define CVPRIO 2
#define KTPRIO 0
#define G2DP 4
#define SB() __builtin_amdgcn_sched_barrier(0)

constexpr int L = 2048, D = 640, NM = 128, F = 1728;
constexpr u32 WS_FLAGK = 2048;
constexpr u32 WS_FLAGS = 4096;
constexpr u32 WS_FLAGH = WS_FLAGS + 2048;
constexpr u32 WS_FLAGY = WS_FLAGH + 2048;
constexpr u32 WS_GATE = 81920;
constexpr u32 WS_KX = 131072;
constexpr u32 WS_UT = WS_KX + 128 * 4096;
constexpr u32 WS_YT = WS_UT + D * L * 2;
constexpr u32 WS_HT = WS_YT + D * L * 2;
constexpr u32 WS_W2T = WS_HT + 32 * 54 * 4096;
constexpr u32 WS_END = WS_W2T + 8 * 54 * 5120;
#define STAMP(k)
constexpr u32 SCR_W = 15616;
constexpr u32 REG_A = 0, REG_B = 65536;
constexpr u32 SLAB = 64 * 336;
constexpr u32 LDS_SIDE = 131072;
constexpr u32 LDS_X1 = REG_B + 24576, LDS_X2 = REG_B + 40960;
constexpr u32 LDS_GW = LDS_SIDE + 20480;
constexpr u32 LDS_B1 = LDS_GW + 2048;
constexpr u32 LDS_PAR = LDS_B1 + 1024;
constexpr u32 LDS_GA = LDS_GW;
constexpr u32 LDS_OMG = LDS_GA + 10240;
constexpr u32 LDS_B2 = LDS_OMG + 256;
constexpr u32 LDS_CTL = LDS_B2 + 320;
constexpr u32 LDS_TOTAL = LDS_CTL + 64;
#define DBG(i)
constexpr u32 CH_KC0 = 0, CH_K = 2576, CH_U = CH_K + 4608, UCP = 4160, CH_KC1 = 2 * SCR_W - 2576;
constexpr u32 LDS_YT = 0;
constexpr u32 CT_PAIR = LDS_CTL, CT_H = LDS_CTL + 16, CT_Y = LDS_CTL + 20, CT_R = LDS_CTL + 24, CT_FH = LDS_CTL + 28, CT_FY = LDS_CTL + 32, CT_FS = LDS_CTL + 36, CT_Q = LDS_CTL + 40, CT_A = LDS_CTL + 48, CT_S = LDS_CTL + 52, CT_FW = LDS_CTL + 56;

struct Params {
  const u16 *u, *lar, *aim, *cre, *cim, *ldt, *dsk, *w1, *b1, *w2, *b2, *gw;
  u16* out;
  char* ws;
};

DEV float bf2f(u32 x) { return __uint_as_float(x << 16); }
DEV float bflo(u32 x) { return __uint_as_float(x << 16); }
DEV float bfhi(u32 x) { return __uint_as_float(x & 0xffff0000u); }
DEV u32 pk_bf16(float a, float b) { f32x2 v = {a, b}; bf16x2 r = __builtin_convertvector(v, bf16x2); return __builtin_bit_cast(u32, r); }
DEV float rbf(float a) { return bflo(pk_bf16(a, a)); }
DEV u32 pk_f16(f32x2 v) { f16x2 r = __builtin_convertvector(v, f16x2); return __builtin_bit_cast(u32, r); }
DEV f32x2 cmul(f32x2 a, f32x2 b) {
  f32x2 t, r;
  asm("v_pk_mul_f32 %0, %1, %2 op_sel:[0,0] op_sel_hi:[0,1]" : "=v"(t) : "v"(a), "v"(b));
  asm("v_pk_fma_f32 %0, %1, %2, %3 op_sel:[1,1,0] op_sel_hi:[1,0,1] neg_lo:[1,0,0]" : "=v"(r) : "v"(a), "v"(b), "v"(t));
  return r;
}
DEV f32x2 cexp_p(float ar, float ai, float p) {
  float e = __builtin_amdgcn_exp2f(ar * p * 1.44269504088896341f);
  float t = ai * p;
  float hi = t * 0.15915494309189535f;
  float lo = __builtin_fmaf(t, 0.15915494309189535f, -hi);
  lo = __builtin_fmaf(t, 6.4206383e-9f, lo);
  float fr = (hi - __builtin_rintf(hi)) + lo;
  return (f32x2){e * __builtin_amdgcn_cosf(fr), e * __builtin_amdgcn_sinf(fr)};
}
#define WAIT_VM(n) asm volatile("s_waitcnt vmcnt(" #n ")" ::: "memory")
#define WAIT_LGKM0() asm volatile("s_waitcnt lgkmcnt(0)" ::: "memory")
DEV void bar() { asm volatile("s_waitcnt lgkmcnt(0)" ::: "memory"); __builtin_amdgcn_s_barrier(); asm volatile("" ::: "memory"); }
DEV void rawbar() { asm volatile("" ::: "memory"); __builtin_amdgcn_s_barrier(); asm volatile("" ::: "memory"); }

DEV void glds1(const void* base, u32 voff, u32 lds) {
  u32 keep;
  asm volatile("s_mov_b32 %0, m0\n\ts_mov_b32 m0, %3\n\ts_nop 0\n\tglobal_load_lds_dwordx4 %1, %2\n\ts_mov_b32 m0, %0"
               : "=&s"(keep) : "v"(voff), "s"(base), "s"(lds) : "memory");
}
DEV void glds1p(const void* p, u32 lds) {
  u32 keep;
  asm volatile("s_mov_b32 %0, m0\n\ts_mov_b32 m0, %2\n\ts_nop 0\n\tglobal_load_lds_dwordx4 %1, off\n\ts_mov_b32 m0, %0"
               : "=&s"(keep) : "v"(p), "s"(lds) : "memory");
}
DEV void glds4(rsrc_t rs, u32 v0, u32 v1, u32 v2, u32 v3, u32 s, u32 step, u32 lds) {
  u32 keep;
  asm volatile("s_mov_b32 %0, m0\n\ts_mov_b32 m0, %10\n\ts_nop 0\n\t"
               "buffer_load_dwordx4 %1, %5, %6 offen lds\n\t"
               "buffer_load_dwordx4 %2, %5, %7 offen offset:1024 lds\n\t"
               "buffer_load_dwordx4 %3, %5, %8 offen offset:2048 lds\n\t"
               "buffer_load_dwordx4 %4, %5, %9 offen offset:3072 lds\n\t"
               "s_mov_b32 m0, %0"
               : "=&s"(keep) : "v"(v0), "v"(v1), "v"(v2), "v"(v3), "s"(rs), "s"(s), "s"(s + step),
                 "s"(s + 2 * step), "s"(s + 3 * step), "s"(lds) : "memory");
}
DEV u32x2 tr_read(u32 a) {
  s16x4 r = __builtin_amdgcn_ds_read_tr16_b64_v4i16((LAS s16x4*)(size_t)a);
  return __builtin_bit_cast(u32x2, r);
}
DEV bf16x8 mk8(u32x2 a, u32x2 b) { u32x4 v = {a.x, a.y, b.x, b.y}; return __builtin_bit_cast(bf16x8, v); }
DEV f32x4 mma(bf16x8 a, bf16x8 b, f32x4 c) { return __builtin_amdgcn_mfma_f32_16x16x32_bf16(a, b, c, 0, 0, 0); }
#define LD(T, a) (*(const LAS T*)(size_t)(a))
#define ST(T, a) (*(LAS T*)(size_t)(a))

template <int SLEEP>
DEV void poll_flags(const char* p, u64 tag) {
#pragma unroll 1
  for (u32 spins = 0; spins < (1u << 18); ++spins) {
    u64 v = __hip_atomic_load((const GAS u64*)p, RLX_AGENT);
    if (__all(v == tag)) break;
    __builtin_amdgcn_s_sleep(SLEEP);
  }
  __builtin_amdgcn_fence(__ATOMIC_ACQUIRE, "wavefront");
  asm volatile("" ::: "memory");
}
DEV void put_flag(char* p, u64 tag) { __hip_atomic_store((GAS u64*)p, tag, RLX_AGENT); }
DEV u32 lds_arrive(u32 a, int lane) {
  WAIT_LGKM0();
  u32 r = 0;
  if (lane == 0) r = __hip_atomic_fetch_add((LAS u32*)(size_t)a, 1u, RLX_WG);
  return __builtin_amdgcn_readfirstlane(r);
}
DEV void lds_wait(u32 a, u32 target) {
#pragma unroll 1
  for (u32 spins = 0; spins < (1u << 22); ++spins) {
    u32 v = __builtin_amdgcn_readfirstlane(__hip_atomic_load((LAS u32*)(size_t)a, RLX_WG));
    if (v >= target) break;
    __builtin_amdgcn_s_sleep(1);
  }
  asm volatile("" ::: "memory");
}

DEV void kgen(u32 pp, float ldt, u32 (&pk)[57], float& kscale) {
  const float dt = rbf(expf(ldt));
  const float ar = -rbf(expf(bf2f(LD(u16, pp)))) * dt, ai = bf2f(LD(u16, pp + 128)) * dt;
  const float cr = 2.f * bf2f(LD(u16, pp + 256)) * dt, ci = 2.f * bf2f(LD(u16, pp + 384)) * dt;
  u32 mx = max(__float_as_uint(cr) & 0x7fffffffu, __float_as_uint(ci) & 0x7fffffffu);
  mx = max(mx, (u32)__builtin_amdgcn_update_dpp(0, mx, 0x128, 0xf, 0xf, false));
  mx = max(mx, (u32)__builtin_amdgcn_update_dpp(0, mx, 0x124, 0xf, 0xf, false));
  mx = max(mx, (u32)__builtin_amdgcn_update_dpp(0, mx, 0x122, 0xf, 0xf, false));
  mx = max(mx, (u32)__builtin_amdgcn_update_dpp(0, mx, 0x121, 0xf, 0xf, false));
  u32 eb = max(max((u32)__builtin_amdgcn_readlane(mx, 0), (u32)__builtin_amdgcn_readlane(mx, 16)),
               max((u32)__builtin_amdgcn_readlane(mx, 32), (u32)__builtin_amdgcn_readlane(mx, 48))) >> 23;
  eb = eb < 40u ? 40u : (eb > 230u ? 230u : eb);
  const float inv = __uint_as_float((127u + 127u + 12u - eb) << 23);
  kscale = __uint_as_float((eb - 12u - 14u) << 23);
  const f32x2 e1 = cexp_p(ar, ai, 1.f), e8 = cexp_p(ar, -ai, 8.f), e128 = cexp_p(ar, ai, 128.f);
  {
    f32x2 G[16];
    const f32x2 e16 = cmul(e8, e8), e32 = cmul(e16, e16), e64 = cmul(e32, e32);
    G[0] = (f32x2){cr * inv, -ci * inv};
    G[1] = cmul(G[0], e8);
#pragma unroll
    for (int j = 0; j < 2; ++j) G[2 + j] = cmul(G[j], e16);
#pragma unroll
    for (int j = 0; j < 4; ++j) G[4 + j] = cmul(G[j], e32);
#pragma unroll
    for (int j = 0; j < 8; ++j) G[8 + j] = cmul(G[j], e64);
#pragma unroll
    for (int j = 0; j < 16; ++j) pk[j] = pk_f16(G[j]);
  }
  f32x2 Pw[8], Q[17];
  const f32x2 e2 = cmul(e1, e1), e4 = cmul(e2, e2);
  Pw[1] = e1; Pw[2] = e2; Pw[3] = cmul(e1, e2); Pw[4] = e4; Pw[5] = cmul(e1, e4); Pw[6] = cmul(e2, e4); Pw[7] = cmul(Pw[3], e4);
  const f32x2 e256 = cmul(e128, e128), e512 = cmul(e256, e256), e1024 = cmul(e512, e512);
  Q[1] = e128 * 16384.f; Q[2] = e256 * 16384.f; Q[4] = e512 * 16384.f; Q[8] = e1024 * 16384.f;
  Q[3] = cmul(Q[1], e256);
  Q[5] = cmul(Q[1], e512); Q[6] = cmul(Q[2], e512); Q[7] = cmul(Q[3], e512);
#pragma unroll
  for (int j = 1; j < 9; ++j) Q[8 + j] = cmul(Q[j], e1024);
  pk[16] = 0x00007400u;
#pragma unroll
  for (int j = 1; j < 8; ++j) pk[16 + j] = pk_f16(Pw[j] * 16384.f);
  pk[24] = pk_f16(Q[1]);
#pragma unroll
  for (int j = 1; j < 8; ++j) pk[24 + j] = pk_f16(cmul(Q[1], Pw[j]));
#pragma unroll
  for (int i = 2; i < 4; ++i) {
    pk[32 + 4 * (i - 2)] = pk_f16(Q[i]);
#pragma unroll
    for (int j = 1; j < 4; ++j) pk[32 + 4 * (i - 2) + j] = pk_f16(cmul(Q[i], Pw[2 * j]));
  }
#pragma unroll
  for (int i = 4; i < 8; ++i) { pk[40 + 2 * (i - 4)] = pk_f16(Q[i]); pk[41 + 2 * (i - 4)] = pk_f16(cmul(Q[i], Pw[4])); }
#pragma unroll
  for (int i = 8; i < 17; ++i) pk[48 + i - 8] = pk_f16(Q[i]);
}
DEV void ktable(u32 scr, u32 kb, const u32 (&pk)[57], float kscale, int lane) {
  const int l15 = lane & 15, q4 = lane >> 4;
#pragma unroll
  for (int j = 0; j < 57; ++j) ST(u32, scr + lane * 4 + j * 272) = pk[j];
  asm volatile("" ::: "memory");
  {
    f32x4 kc[3];
#pragma unroll
    for (int T = 0; T < 3; ++T) kc[T] = (f32x4){0.f, 0.f, 0.f, 0.f};
    const u32 rp = scr + l15 * 272 + q4 * 16;
    const u32 rp2 = scr + (48 + (l15 > 8 ? 8 : l15)) * 272 + q4 * 16;
    f16x8 fr[4][4];
#pragma unroll
    for (int ks = 0; ks < 4; ++ks) {
      fr[ks][0] = LD(f16x8, rp + ks * 64); fr[ks][1] = LD(f16x8, rp + 16 * 272 + ks * 64);
      fr[ks][2] = LD(f16x8, rp + 32 * 272 + ks * 64); fr[ks][3] = LD(f16x8, rp2 + ks * 64);
    }
    SB();
#pragma unroll
    for (int ks = 0; ks < 4; ++ks)
#pragma unroll
      for (int T = 0; T < 3; ++T) kc[T] = __builtin_amdgcn_mfma_f32_16x16x32_f16(fr[ks][1 + T], fr[ks][0], kc[T], 0, 0, 0);
    WAIT_LGKM0();
    ST(f32x4, kb + (128 * (q4 >> 1) + 8 * l15 + 4 * (q4 & 1)) * 4) = kc[0] * kscale;
    const f32x4 v1 = kc[1] * kscale, v2 = kc[2] * kscale;
    if (q4 < 2) {
      ST(f32x4, kb + (256 + 64 * q4 + 4 * l15) * 4) = v1;
#pragma unroll
      for (int rr = 0; rr < 4; ++rr) ST(float, kb + (512 + 16 * (4 * q4 + rr) + l15) * 4) = v2[rr];
    } else {
      ST(f32x2, kb + (384 + 64 * (q4 - 2) + 2 * l15) * 4) = (f32x2){v1[0], v1[1]};
      ST(f32x2, kb + (416 + 64 * (q4 - 2) + 2 * l15) * 4) = (f32x2){v1[2], v1[3]};
      if (lane == 32) ST(float, kb + 640 * 4) = v2[0];
    }
  }
}
DEV void expand(u32 chb, int half, int lane, u32x4& c0, u32x4& c1) {
  {
    auto tab = [&](int i) { return LD(float, chb + CH_KC0 + i * 4) + LD(float, chb + CH_KC1 + i * 4); };
    const int m = 255 - 64 * half - lane;
    const float a = tab(512 + m - 127), b = tab(512 + m - 128), c = (m & 1) ? a : b;
    const u32 bb = pk_bf16(b, b);
    c0 = (u32x4){pk_bf16(a, a), pk_bf16(a, c), bb, bb};
    if (half == 0) {
      const int i = 384 + 2 * (63 - lane);
      const float x = tab(i + 2), y = tab(i + 1), z = tab(i);
      c1 = (u32x4){pk_bf16(x, x), pk_bf16(y, y), pk_bf16(y, z), pk_bf16(z, z)};
    } else {
      const bool lo = lane >= 32;
      const int i = lo ? 8 * (63 - lane) : 256 + 4 * (31 - lane);
      const f32x4 s0 = LD(f32x4, chb + CH_KC0 + i * 4) + LD(f32x4, chb + CH_KC1 + i * 4);
      const f32x4 s1 = LD(f32x4, chb + CH_KC0 + i * 4 + 16) + LD(f32x4, chb + CH_KC1 + i * 4 + 16);
      c1 = (u32x4){pk_bf16(lo ? s1[3] : s1[0], lo ? s1[2] : s0[3]), pk_bf16(lo ? s1[1] : s0[2], lo ? s1[0] : s0[2]),
                   pk_bf16(lo ? s0[3] : s0[2], lo ? s0[2] : s0[1]), pk_bf16(lo ? s0[1] : s0[0], s0[0])};
    }
  }
}

struct ConvFrags { bf16x8 A[2][8], B[2][8]; };
template <bool NEXT>
DEV void conv_step(ConvFrags& f, int set, u32 pa, u32 pa8, u32 pb, f32x4 (&acc)[8]) {
  u32x2 lo[8], hi[8];
#pragma unroll
  for (int g = 0; g < 12; ++g) {
    if (NEXT) {
#pragma unroll
      for (int d = 2 * g; d < 2 * g + 2; ++d) {
        const int k = d / 3;
        if (d % 3 == 0) lo[k] = LD(u32x2, pa + 512 * k);
        if (d % 3 == 1) { hi[k] = LD(u32x2, pa8 + 512 * k); f.A[set ^ 1][k] = mk8(lo[k], hi[k]); }
        if (d % 3 == 2) f.B[set ^ 1][7 - k] = LD(bf16x8, pb + 512 * k);
      }
    }
#pragma unroll
    for (int j = 3 * g; j < 3 * g + 3; ++j) {
      const int k = j < 1 ? 0 : j < 3 ? 1 : j < 6 ? 2 : j < 10 ? 3 : j < 15 ? 4 : j < 21 ? 5 : j < 28 ? 6 : 7, e = j - k * (k + 1) / 2;
      acc[e + 7 - k] = mma(f.A[set][e], f.B[set][7 - k], acc[e + 7 - k]);
    }
    if (NEXT) {
#pragma unroll
      for (int j = 0; j < 2; ++j) { __builtin_amdgcn_sched_group_barrier(0x8, 1, 0); __builtin_amdgcn_sched_group_barrier(0x100, 1, 0); }
      __builtin_amdgcn_sched_group_barrier(0x8, 1, 0);
    }
    SB();
  }
}

__global__ __launch_bounds__(512, 2) void fused(Params P) {
  extern __shared__ __attribute__((aligned(16))) char smem[];
  const int tid = threadIdx.x, lane = tid & 63;
  const int wave = __builtin_amdgcn_readfirstlane(tid >> 6);
  const int bid = blockIdx.x, xcd = bid & 7, r = bid >> 3;
  const bool regular = r < 28;
  const int quad = (wave >> 2) ^ 1, qw = wave & 3, half = qw & 1;
  const int l15 = lane & 15, q4 = lane >> 4;
  const u32 lds0 = (u32)(size_t)(LAS char*)smem;
  char* ws = P.ws;
  const int rq = (r * 37) >> 8, nl = r - 7 * rq, mi = (xcd >> 1) * 4 + rq, ni = (xcd & 1) * 7 + nl;
  const int rot = nl + 7 * rq - 10 * ((nl + 7 * rq) / 10);
  const int m64 = (xcd >> 1) * 8 + (r >> 2), n80 = (xcd & 1) * 4 + (r & 3);
  const rsrc_t wsr = __builtin_amdgcn_make_buffer_rsrc(ws, 0, WS_END, 0x00020000);
  const int chan0 = 80 * xcd + 2 * r;
  const u32 chb = lds0 + REG_A + (qw >> 1) * (2 * SCR_W);
  const int ch = chan0 + (qw >> 1);
  const bool hasx = !(r & 1);
  u64 tag;

  auto par_load = [&](int ch, u32 dst) {
    const int a = (lane >> 3) & 3;
    const u16 *p0 = P.lar, *p1 = P.aim, *p2 = P.cre, *p3 = P.cim;
    asm volatile("" : "+s"(p0), "+s"(p1), "+s"(p2), "+s"(p3));
    const u16* p = a == 0 ? p0 : a == 1 ? p1 : a == 2 ? p2 : p3;
    if (lane < 32) glds1p(p + ch * NM + 64 * half + (lane & 7) * 8, dst);
  };
  const bool isw = qw >= 2, side = !isw || m64 < 27;
  auto side_load = [&]() {
#pragma unroll
    for (int i = 0; i < 5; ++i) {
      const int o = i * 64 + lane, row = (o * 6554) >> 16, c = o - 10 * row;
      glds1(isw ? P.w2 : P.u, (u32)((m64 * 64 + half * 32 + row) * (D * 2) + n80 * 160 + c * 16), lds0 + LDS_SIDE + qw * 5120 + i * 1024);
    }
  };
  auto side_store = [&]() {
    const u32 p = lds0 + LDS_SIDE + qw * 5120 + ((isw ? 4 : 8) * q4 + (l15 >> 2)) * 160 + 8 * (lane & 3), p2 = p + (isw ? 16 * 160 : 4 * 160);
    const u32 o = isw ? WS_W2T + (u32)((n80 * 54 + 2 * m64 + half) * 5120) + lane * 16
                      : WS_UT + (u32)((80 * n80 + l15) * (L * 2) + (64 * m64 + 32 * half) * 2 + q4 * 16);
    u32x2 a[5], b[5];
#pragma unroll
    for (int i = 0; i < 5; ++i) { a[i] = tr_read(p + 32 * i); b[i] = tr_read(p2 + 32 * i); }
#pragma unroll
    for (int i = 0; i < 5; ++i)
      __builtin_amdgcn_raw_buffer_store_b128((u32x4){a[i].x, a[i].y, b[i].x, b[i].y}, wsr, o + i * (isw ? 1024 : 16 * L * 2), 0, 16);
  };

  u32 ldt2 = 0;
  if (!regular) {
    const int xi = 4 * (r - 28) + 2 * quad + (qw >> 1);
    par_load(80 * xcd + 64 + xi, lds0 + LDS_PAR + (4 + wave) * 512);
    if (quad == 0) par_load(chan0 + (qw >> 1), lds0 + LDS_PAR + qw * 512);
    if (quad == 1 && side) side_load();
    if (tid < 16) ST(u32, lds0 + LDS_CTL + tid * 4) = 0;
    u32 ldx;
    asm volatile("s_load_dwordx2 %0, %3, 0x0\n\ts_load_dword %1, %4, 0x0\n\ts_load_dword %2, %5, 0x0\n\ts_waitcnt lgkmcnt(0)"
                 : "=&s"(tag), "=&s"(ldt2), "=&s"(ldx) : "s"(ws), "s"(P.ldt + chan0), "s"(P.ldt + 80 * xcd + 64 + (xi & ~1)) : "memory");
    tag += 1;
    if (quad == 1 && side) WAIT_VM(5); else WAIT_VM(0);
    u32 pk[57];
    float kscale;
    kgen(lds0 + LDS_PAR + (4 + wave) * 512 + lane * 2, (xi & 1) ? bfhi(ldx) : bflo(ldx), pk, kscale);
#pragma unroll
    for (int j = 0; j < 57; ++j) asm volatile("" : "+v"(pk[j]));
    WAIT_VM(0);
    STAMP(1);
    if (quad == 1 && side) side_store();
    const u32 xb = chb + (quad ? REG_B : 0);
    ktable(xb + half * SCR_W, xb + (half ? CH_KC1 : CH_KC0), pk, kscale, lane);
    bar();
    u32x4 c0, c1;
    expand(xb, half, lane, c0, c1);
    const u32 ko = WS_KX + (u32)((xcd * 16 + xi) * 4096 + half * 1024) + lane * 16;
    __builtin_amdgcn_raw_buffer_store_b128(c0, wsr, ko, 0, 16);
    __builtin_amdgcn_raw_buffer_store_b128(c1, wsr, ko + 2048, 0, 16);
    WAIT_VM(0);
    bar();
    if (tid == 0) { put_flag(ws + WS_FLAGK + (xcd * 4 + r - 28) * 8, tag); put_flag(ws + WS_FLAGS + (n80 * 32 + m64) * 8, tag); }
    STAMP(2);
  }

  if (quad == 0) {
    const rsrc_t xr = __builtin_amdgcn_make_buffer_rsrc((void*)P.u, 0, L * D * 2, 0x00020000);
    const rsrc_t wr = __builtin_amdgcn_make_buffer_rsrc((void*)P.w1, 0, D * F * 2, 0x00020000);
    const u32 xv = (u32)((lane >> 3) * (D * 2) + ((lane & 7) ^ q4) * 16);
    const u32 wv = (u32)(q4 * (F * 2) + (l15 ^ (q4 << 2)) * 16);
    const u32 xs = (u32)((mi * 128 + 32 * qw) * (D * 2)), wso = (u32)(16 * qw * (F * 2) + ni * 256);
    auto stage = [&](int i) {
      const u32 dst = lds0 + ((i + 2) & 3) * 32768 + qw * 4096;
      const int k = i + rot >= 10 ? i + rot - 10 : i + rot;
      glds4(xr, xv, xv ^ 64, xv, xv ^ 64, xs + k * 128, 8 * D * 2 - 1024, dst);
      glds4(wr, wv, wv ^ 16, wv ^ 32, wv ^ 48, wso + k * (64 * F * 2), 4 * F * 2 - 1024, dst + 16384);
    };
    if (regular) {
      __builtin_amdgcn_s_setprio(3);
      rawbar();
      par_load(chan0 + (qw >> 1), lds0 + LDS_PAR + qw * 512);
      stage(0);
      if (qw == 0) glds1(P.b1, (u32)(ni * 256 + (lane & (ni == 13 ? 7 : 15)) * 16), lds0 + LDS_B1);
      if (qw == 1 && ni == 13) { glds1(P.gw, lane * 16, lds0 + LDS_GW); glds1(P.gw, 1024 + l15 * 16, lds0 + LDS_GW + 1024); }
      stage(1);
      asm volatile("s_load_dwordx2 %0, %2, 0x0\n\ts_load_dword %1, %3, 0x0\n\ts_waitcnt lgkmcnt(0)"
                   : "=&s"(tag), "=&s"(ldt2) : "s"(ws), "s"(P.ldt + chan0) : "memory");
      tag += 1;
      WAIT_VM(16);
    }
    u32 pk[57];
    float kscale;
    kgen(lds0 + LDS_PAR + qw * 512 + lane * 2, (qw >> 1) ? bfhi(ldt2) : bflo(ldt2), pk, kscale);
#pragma unroll
    for (int j = 0; j < 57; ++j) asm volatile("" : "+v"(pk[j]));
    STAMP(5);
    if (regular) {
      stage(2);
#pragma unroll 1
      for (int s = 0; s < 7; ++s) {
        WAIT_VM(8);
        rawbar();
        stage(s + 3);
        if (s == 3 && tid == 256) put_flag(ws + WS_FLAGS + (n80 * 32 + m64) * 8, tag);
      }
      WAIT_VM(8);
      rawbar();
      WAIT_VM(0);
      rawbar();
    }
    STAMP(4);
    __builtin_amdgcn_s_setprio(KTPRIO);
    if (qw == 0) {
      if (!regular) __builtin_amdgcn_s_sleep(HSLEEP);
      poll_flags<12>(lane < 32 ? ws + WS_FLAGS + (xcd * 32 + lane) * 8 : lane < 59 ? ws + WS_FLAGS + (n80 * 32 + lane - 32) * 8
                                                                                   : ws + WS_FLAGK + (xcd * 4 + (r >> 3)) * 8, tag);
      if (lane == 0) __hip_atomic_store((LAS u32*)(size_t)(lds0 + CT_FS), 1u, RLX_WG);
    } else {
      lds_wait(lds0 + CT_FS, 1u);
    }
    {
      u32x4 ud[3], kx;
      u32x2 un[3];
      const u32 uo = WS_UT + (u32)(ch * (L * 2) + half * 2048);
#pragma unroll
      for (int i = 0; i < 2; ++i) {
        ud[i] = __builtin_amdgcn_raw_buffer_load_b128(wsr, lane * 16 + i * 1024, uo, 16);
        un[i] = __builtin_amdgcn_raw_buffer_load_b64(wsr, lane * 16 + i * 1024 + 16, uo, 16);
      }
      const u32 xo = WS_UT + (u32)((80 * xcd + 64 + (r >> 1)) * (L * 2) + qw * 1024);
      ud[2] = __builtin_amdgcn_raw_buffer_load_b128(wsr, lane * 16, xo, 16);
      un[2] = __builtin_amdgcn_raw_buffer_load_b64(wsr, lane * 16 + 16, xo, 16);
      kx = __builtin_amdgcn_raw_buffer_load_b128(wsr, lane * 16, WS_KX + (u32)((xcd * 16 + (r >> 1)) * 4096 + qw * 1024), 16);
      STAMP(1);
      ktable(chb + half * SCR_W, chb + (half ? CH_KC1 : CH_KC0), pk, kscale, lane);
      lds_arrive(lds0 + CT_PAIR + (qw >> 1) * 4, lane);
      lds_wait(lds0 + CT_PAIR + (qw >> 1) * 4, 2);
      u32x4 c0, c1;
      expand(chb, half, lane, c0, c1);
      ST(u32x4, chb + CH_K + (64 * half + lane) * 16) = c0;
      ST(u32x4, chb + CH_K + (128 + 64 * half + lane) * 16) = c1;
      auto put4 = [&](u32 a, u32x4 d, u32x2 n) {
        const u32 s0 = __builtin_amdgcn_alignbit(d[1], d[0], 16), s1 = __builtin_amdgcn_alignbit(d[2], d[1], 16),
                  s2 = __builtin_amdgcn_alignbit(d[3], d[2], 16), s3 = __builtin_amdgcn_alignbit(n[0], d[3], 16),
                  s4 = __builtin_amdgcn_alignbit(n[1], n[0], 16);
        ST(u32x4, a) = d;
        ST(u32x4, a + UCP) = (u32x4){s0, s1, s2, s3};
        ST(u32x4, a + 2 * UCP) = (u32x4){d[1], d[2], d[3], n[0]};
        ST(u32x4, a + 3 * UCP) = (u32x4){s1, s2, s3, s4};
      };
      auto edges = [&](u32 blk, u32x4 d) {
        const u32x4 z = {0, 0, 0, 0};
        if (lane < 2) put4(blk + CH_U + 16 - lane * 16, z, lane ? (u32x2){0, 0} : (u32x2){d[0], d[1]});
        else if (lane < 32) ST(u32x4, blk + CH_K + (254 + lane) * 16) = z;
      };
#pragma unroll
      for (int i = 0; i < 2; ++i) put4(chb + CH_U + 32 + half * 2048 + i * 1024 + lane * 16, ud[i], un[i]);
      if (half == 0) edges(chb, ud[0]);
      if (hasx) {
        if (regular) lds_wait(lds0 + CT_R, 4u);
        put4(lds0 + REG_B + CH_U + 32 + qw * 1024 + lane * 16, ud[2], un[2]);
        ST(u32x4, lds0 + REG_B + CH_K + qw * 1024 + lane * 16) = kx;
        if (qw == 0) edges(lds0 + REG_B, ud[2]);
      }
    }
    lds_arrive(lds0 + CT_Q, lane);
    STAMP(8);
    __builtin_amdgcn_s_setprio(G2PRIO);
    if (regular) lds_wait(lds0 + CT_R, 4u);
    constexpr int NS = 14, DP = G2DP;
    const int first = qw >> 1;
    const int k0 = (qw < 2 ? 14 * qw : 28 + 13 * (qw - 2)) - first;
    const u32 hb = WS_HT + (u32)((m64 * 54 + k0) * 4096), wb = WS_W2T + (u32)((n80 * 54 + k0) * 5120);
    const u32 lo = lane * 16;
    u32x4 fr[DP][9];
    auto load1 = [&](int s, int i) {
      const int j = s;
      fr[s % DP][i] = i < 5 ? __builtin_amdgcn_raw_buffer_load_b128(wsr, lo + (i & 3) * 1024, wb + j * 5120 + (i >> 2) * 4096, 16)
                            : __builtin_amdgcn_raw_buffer_load_b128(wsr, lo + (i - 5) * 1024, hb + j * 4096, 16);
    };
    if (first == 0) {
#pragma unroll
      for (int i = 0; i < 5; ++i) load1(0, i);
    }
#pragma unroll
    for (int i = 0; i < 5 * (DP - 2); ++i) load1(1 + i / 5, i % 5);
    const u32 tch = __builtin_amdgcn_raw_buffer_load_b32(wsr, (u32)(((m64 & 7) * 270 + qw * 64 + lane) * 128), WS_W2T + (u32)(n80 * 54 * 5120), 16);
    STAMP(6);
    if (qw == 0) {
      if (regular) lds_wait(lds0 + CT_H, 4u);
      poll_flags<4>(ws + WS_FLAGH + ((m64 >> 1) * 16 + (lane < 14 ? lane : 0)) * 8, tag);
      if (lane == 0) __hip_atomic_store((LAS u32*)(size_t)(lds0 + CT_FH), 1u, RLX_WG);
    } else {
      lds_wait(lds0 + CT_FH, 1u);
    }
    asm volatile("" :: "v"(tch));
    STAMP(11);
    if (first == 0) {
#pragma unroll
      for (int i = 5; i < 9; ++i) load1(0, i);
    }
#pragma unroll
    for (int i = 0; i < 4 * (DP - 2); ++i) load1(1 + i / 4, 5 + i % 4);
    f32x4 acc[5][4];
#pragma unroll
    for (int a = 0; a < 5; ++a)
#pragma unroll
      for (int b = 0; b < 4; ++b) acc[a][b] = (f32x4){0.f, 0.f, 0.f, 0.f};
    SB();
#pragma unroll
    for (int s = 0; s < NS; ++s) {
      if (s > 0 || first == 0) {
#pragma unroll
        for (int a = 0; a < 5; ++a) {
          if (s + DP - 1 < NS) { load1(s + DP - 1, 2 * a); if (a < 4) load1(s + DP - 1, 2 * a + 1); }
#pragma unroll
          for (int b = 0; b < 4; ++b)
            acc[a][b] = mma(__builtin_bit_cast(bf16x8, fr[s % DP][a]), __builtin_bit_cast(bf16x8, fr[s % DP][5 + b]), acc[a][b]);
          SB();
        }
      } else {
#pragma unroll
        for (int i = 0; i < 9; ++i) load1(DP - 1, i);
        SB();
      }
    }
    STAMP(12);
    lds_wait(lds0 + CT_Y, 4u);
    const u32 sl = lds0 + REG_B + qw * SLAB + l15 * 336 + q4 * 16;
#pragma unroll
    for (int a = 0; a < 5; ++a)
#pragma unroll
      for (int b = 0; b < 4; ++b) ST(f32x4, sl + b * 16 * 336 + a * 64) = acc[a][b];
  } else {
    const bool lastn = ni == 13;
    if (regular) {
      if (side) side_load();
      rawbar();
      if (tid < 16) ST(u32, lds0 + LDS_CTL + tid * 4) = 0;
      asm volatile("s_load_dwordx2 %0, %1, 0x0\n\ts_waitcnt lgkmcnt(0)" : "=&s"(tag) : "s"(ws) : "memory");
      tag += 1;
      WAIT_VM(0);
      STAMP(5);
      if (side) side_store();
      STAMP(6);
      __builtin_amdgcn_s_setprio(2);
      const int wf = qw & 1, wt = qw >> 1;
      u32 xa[2], wa[4][2];
      xa[0] = lds0 + 65536 + (u32)((64 * wt + l15) * 128 + ((q4 ^ (l15 >> 1)) * 16)); xa[1] = xa[0] ^ 64;
      f32x4 acc[4][4];
      if (!(lastn && wf)) {
#pragma unroll
        for (int t = 0; t < 2; ++t) {
          const int qq = l15 >> 2, p = lane & 3;
          const int row = 8 * q4 + 4 * t + qq, c = 8 * wf + (p >> 1);
          const u32 a0 = lds0 + 65536 + 16384u + 256u * row + 16u * (c ^ ((qq << 2) | ((2 * q4 + t) & 3))) + 8u * (p & 1);
#pragma unroll
          for (int ft = 0; ft < 4; ++ft) wa[ft][t] = a0 ^ (ft << 5);
        }
        bf16x8 A[2][4], B[2][4];
        auto rdf = [&](int set, int i, u32 so, int kk) {
          A[set][i] = mk8(tr_read(wa[i][0] + so + kk * 8192), tr_read(wa[i][1] + so + kk * 8192));
          B[set][i] = LD(bf16x8, xa[kk] + so + i * 2048);
        };
        auto hstep = [&](int set, u32 so, int kk) {
          constexpr int ord[16][2] = {{0, 0}, {0, 1}, {1, 0}, {1, 1}, {0, 2}, {1, 2}, {2, 0}, {2, 1},
                                      {2, 2}, {0, 3}, {1, 3}, {2, 3}, {3, 0}, {3, 1}, {3, 2}, {3, 3}};
#pragma unroll
          for (int g = 0; g < 4; ++g) {
            rdf(set ^ 1, g, so, kk);
#pragma unroll
            for (int j = 0; j < 4; ++j) {
              const int a = ord[4 * g + j][0], b = ord[4 * g + j][1];
              acc[a][b] = mma(A[set][a], B[set][b], acc[a][b]);
            }
#pragma unroll
            for (int j = 0; j < 3; ++j) { __builtin_amdgcn_sched_group_barrier(0x8, 1, 0); __builtin_amdgcn_sched_group_barrier(0x100, 1, 0); }
            __builtin_amdgcn_sched_group_barrier(0x8, 1, 0);
            SB();
          }
        };
        bar();
        STAMP(1);
#pragma unroll
        for (int i = 0; i < 4; ++i) rdf(0, i, 0, 0);
#pragma unroll
        for (int ft = 0; ft < 4; ++ft) {
          const u32x2 bi = LD(u32x2, lds0 + LDS_B1 + (wf * 64 + ft * 16 + 4 * q4) * 2);
#pragma unroll
          for (int tt = 0; tt < 4; ++tt) acc[ft][tt] = (f32x4){bflo(bi.x), bfhi(bi.x), bflo(bi.y), bfhi(bi.y)};
        }
#pragma unroll 1
        for (int p = 0; p < 5; ++p) {
          DBG(4 * p); if (p > 0) rawbar(); DBG(4 * p + 1);
          SB();
          hstep(0, 0, 1); hstep(1, 32768, 0);
          if (p == 1) WAIT_VM(0);
          if (p < 4) rawbar();
          DBG(4 * p + 3);
          SB();
          hstep(0, 32768, 1);
          xa[0] ^= 65536; xa[1] ^= 65536;
#pragma unroll
          for (int ft = 0; ft < 4; ++ft) { wa[ft][0] ^= 65536; wa[ft][1] ^= 65536; }
          SB();
          hstep(1, 0, 0);
        }
        STAMP(2);
        const u32 ho = WS_HT + (u32)(((2 * mi + wt) * 54 + 4 * ni + 2 * wf) * 4096);
#pragma unroll
        for (int kh = 0; kh < 2; ++kh)
#pragma unroll
          for (int tt = 0; tt < 4; ++tt) {
            u32x4 o;
#pragma unroll
            for (int h = 0; h < 2; ++h) {
              f32x4 v = __builtin_bit_cast(f32x4, __builtin_elementwise_max(__builtin_bit_cast(i32x4, acc[2 * kh + h][tt]), (i32x4){0, 0, 0, 0}));
              v = v * v;
              o[2 * h] = pk_bf16(v[0], v[1]); o[2 * h + 1] = pk_bf16(v[2], v[3]);
            }
            __builtin_amdgcn_raw_buffer_store_b128(o, wsr, ho + kh * 4096 + lane * 16 + tt * 1024, 0, 16);
          }
      } else {
#pragma unroll
        for (int tt = 0; tt < 4; ++tt) acc[0][tt] = (f32x4){0.f, 0.f, 0.f, 0.f};
#pragma unroll 1
        for (int s = 0; s < 10; ++s) {
          if (s == 3) WAIT_VM(0);
          if (s < 9) bar();
          const int k = s + rot >= 10 ? s + rot - 10 : s + rot;
#pragma unroll
          for (int kk = 0; kk < 2; ++kk) {
            const bf16x8 Ag = LD(bf16x8, lds0 + LDS_GW + k * 128 + kk * 64 + q4 * 16);
#pragma unroll
            for (int tt = 0; tt < 4; ++tt) acc[0][tt] = mma(Ag, LD(bf16x8, (xa[kk] ^ (((s + 2) & 3) * 32768) ^ 65536) + tt * 2048), acc[0][tt]);
          }
        }
        float pre = q4 == 0 ? acc[0][0][0] : q4 == 1 ? acc[0][1][0] : q4 == 2 ? acc[0][2][0] : acc[0][3][0];
        pre = rbf(pre);
        const float g = rbf(1.f / (1.f + __expf(-pre)));
        __builtin_amdgcn_raw_buffer_store_b32(__float_as_uint(g), wsr, WS_GATE + (u32)(mi * 128 + 64 * wt + lane) * 4, 0, 16);
      }
      if (lane == 0) __hip_atomic_fetch_add((LAS u32*)(size_t)(lds0 + CT_R), 1u, RLX_WG);
      STAMP(3);
    }
    __builtin_amdgcn_s_setprio(CVPRIO);
    if (regular) {
      WAIT_VM(0);
      if (lds_arrive(lds0 + CT_H, lane) == 3 && lane == 0) put_flag(ws + WS_FLAGH + (mi * 16 + ni) * 8, tag);
      STAMP(4);
    }
    {
      u32x4 uu[3], dd[3];
#pragma unroll
      for (int i = 0; i < 3; ++i) {
        const int c = 4 * i + q4 < 10 ? 4 * i + q4 : 9;
        uu[i] = *(const u32x4*)(P.u + (size_t)(64 * m64 + 16 * qw + l15) * D + 80 * n80 + 8 * c);
        dd[i] = *(const u32x4*)(P.dsk + 80 * n80 + 8 * c);
      }
      const u32 b2v = P.b2[80 * n80 + (tid < 80 ? tid : 0)];
      if (regular) {
        lds_wait(lds0 + CT_R, 4u);
      } else {
        lds_wait(lds0 + CT_PAIR, 2); lds_wait(lds0 + CT_PAIR + 4, 2);
      }
#pragma unroll
      for (int i = 0; i < 3; ++i) {
        const int c = 4 * i + q4 < 10 ? 4 * i + q4 : 9;
        u32x4 v;
#pragma unroll
        for (int e = 0; e < 4; ++e) v[e] = pk_bf16(bflo(uu[i][e]) * bflo(dd[i][e]), bfhi(uu[i][e]) * bfhi(dd[i][e]));
        ST(u32x4, lds0 + LDS_GA + (16 * qw + l15) * 160 + c * 16) = v;
      }
      if (tid < 80) ST(float, lds0 + LDS_B2 + tid * 4) = bf2f(b2v);
    }
    lds_wait(lds0 + CT_Q, 4u);
    STAMP(8);
    {
      f32x4 acc[8], acx[8];
#pragma unroll
      for (int j = 0; j < 8; ++j) { acc[j] = (f32x4){0.f, 0.f, 0.f, 0.f}; acx[j] = acc[j]; }
      const int z = l15 + 8 * q4 + 1;
      const u32 oa = CH_U + (z & 3) * UCP + (z & ~3) * 2, ob = CH_K + (2032 - 16 * l15 + 8 * q4) * 2 - 512 * 7;
      const u32 b0 = chb + half * 256, b1 = lds0 + REG_B + qw * 128;
      u32 pa = b0 + oa, pa8 = pa + 8, pb = b0 + ob, xa = b1 + oa, xa8 = xa + 8, xb = b1 + ob;
      asm volatile("" : "+v"(pa8), "+v"(xa8));
      ConvFrags f;
#pragma unroll
      for (int k = 0; k < 8; ++k) { f.A[0][k] = mk8(LD(u32x2, pa + 512 * k), LD(u32x2, pa8 + 512 * k)); f.B[0][7 - k] = LD(bf16x8, pb + 512 * k); }
      SB();
      conv_step<true>(f, 0, pa + 64, pa8 + 64, pb + 64, acc);
      conv_step<true>(f, 1, pa + 128, pa8 + 128, pb + 128, acc);
      conv_step<true>(f, 0, pa + 192, pa8 + 192, pb + 192, acc);
      conv_step<true>(f, 1, xa, xa8, xb, acc);
      if (hasx) {
        conv_step<true>(f, 0, xa + 64, xa8 + 64, xb + 64, acx);
        conv_step<false>(f, 1, 0, 0, 0, acx);
      }
      STAMP(9);
      if (half) {
#pragma unroll
        for (int j = 0; j < 4; ++j) { const f32x4 t = acc[j]; acc[j] = acc[4 + j]; acc[4 + j] = t; asm volatile("" : "+v"(acc[j]), "+v"(acc[4 + j])); }
      }
      const u32 x1 = lds0 + LDS_X1 + lane * 16, x2 = lds0 + LDS_X2 + lane * 16;
#pragma unroll
      for (int j = 0; j < 4; ++j) ST(f32x4, x1 + qw * 4096 + j * 1024) = acc[4 + j];
      if (hasx) {
#pragma unroll
        for (int j = 0; j < 8; ++j) ST(f32x4, x2 + qw * 8192 + j * 1024) = acx[j];
      }
      lds_arrive(lds0 + CT_Q, lane);
      lds_wait(lds0 + CT_Q, 8u);
      const u32 yo = (u32)(16 * l15 + 4 * q4) * 2;
#pragma unroll
      for (int j = 0; j < 4; ++j) {
        const f32x4 v = acc[j] + LD(f32x4, x1 + (qw ^ 1) * 4096 + j * 1024);
        __builtin_amdgcn_raw_buffer_store_b64((u32x2){pk_bf16(v[0], v[1]), pk_bf16(v[2], v[3])}, wsr, yo,
                                              WS_YT + (u32)(ch * (L * 2) + (4 * half + j) * 512), 16);
      }
      if (hasx) {
#pragma unroll
        for (int j = 0; j < 2; ++j) {
          const u32 o = x2 + (2 * qw + j) * 1024;
          const f32x4 v = (LD(f32x4, o) + LD(f32x4, o + 8192)) + (LD(f32x4, o + 16384) + LD(f32x4, o + 24576));
          __builtin_amdgcn_raw_buffer_store_b64((u32x2){pk_bf16(v[0], v[1]), pk_bf16(v[2], v[3])}, wsr, yo,
                                                WS_YT + (u32)((80 * xcd + 64 + (r >> 1)) * (L * 2) + (2 * qw + j) * 512), 16);
        }
      }
      WAIT_VM(0);
      if (lds_arrive(lds0 + CT_Y, lane) == 3u && lane == 0) put_flag(ws + WS_FLAGY + (xcd * 32 + r) * 8, tag);
      STAMP(10);
    }
    if (qw == 0) {
      lds_wait(lds0 + CT_Y, 4u);
      poll_flags<8>(lane < 32 ? ws + WS_FLAGY + (n80 * 32 + lane) * 8 : ws + WS_FLAGH + ((m64 >> 1) * 16 + 13) * 8, tag);
      if (lane == 0) __hip_atomic_store((LAS u32*)(size_t)(lds0 + CT_FY), 1u, RLX_WG);
    } else {
      lds_wait(lds0 + CT_FY, 1u);
    }
    STAMP(11);
    const float g = __uint_as_float(__builtin_amdgcn_raw_buffer_load_b32(wsr, (16 * qw + l15) * 4, WS_GATE + (u32)(64 * m64) * 4, 16));
    u32x4 yy[3];
#pragma unroll
    for (int i = 0; i < 3; ++i) {
      const int id = tid + 256 * i < 640 ? tid + 256 * i : 639;
      yy[i] = __builtin_amdgcn_raw_buffer_load_b128(wsr, (id >> 3) * (L * 2) + (id & 7) * 16, WS_YT + (u32)(80 * n80 * (L * 2) + 64 * m64 * 2), 16);
    }
    if (q4 == 0) ST(float, lds0 + LDS_OMG + (16 * qw + l15) * 4) = rbf(1.f - g);
#pragma unroll
    for (int i = 0; i < 3; ++i) {
      const int id = tid + 256 * i < 640 ? tid + 256 * i : 639;
      ST(u32x4, lds0 + LDS_YT + (id >> 3) * 144 + (id & 7) * 16) = yy[i];
    }
    lds_arrive(lds0 + CT_A, lane);
    lds_wait(lds0 + CT_A, 4u);
#pragma unroll
    for (int i = 0; i < 3; ++i) {
      const int c = 4 * i + q4 < 10 ? 4 * i + q4 : 9;
      const u32 p = lds0 + LDS_YT + (8 * c + (l15 >> 2)) * 144 + qw * 32 + 8 * (lane & 3);
      const u32x2 ya = tr_read(p), yb = tr_read(p + 4 * 144);
      const u32 ga = lds0 + LDS_GA + (16 * qw + l15) * 160 + c * 16;
      u32x4 z = LD(u32x4, ga);
#pragma unroll
      for (int e = 0; e < 4; ++e) {
        const u32 y = e < 2 ? ya[e] : yb[e - 2];
        z[e] = pk_bf16(g * (bflo(y) + bflo(z[e])), g * (bfhi(y) + bfhi(z[e])));
      }
      if (4 * i + q4 < 10) ST(u32x4, ga) = z;
    }
    if (bid == 0 && wave == 3) {
#pragma unroll 1
      for (int i = 0; i < 4; ++i) poll_flags<16>(ws + WS_FLAGY + (i * 64 + lane) * 8, tag);
      if (lane == 0) put_flag(ws, tag);
    }
  }
  STAMP(13);
  bar();
  STAMP(14);
#pragma unroll 1
  for (int id = tid; id < 640; id += 512) {
    const int t = (id * 6554) >> 16, c8 = id - 10 * t;
    f32x4 s0 = LD(f32x4, lds0 + LDS_B2 + c8 * 32), s1 = LD(f32x4, lds0 + LDS_B2 + c8 * 32 + 16);
#pragma unroll
    for (int v = 0; v < 4; ++v) {
      const u32 p = lds0 + REG_B + v * SLAB + t * 336 + c8 * 32;
      s0 += LD(f32x4, p); s1 += LD(f32x4, p + 16);
    }
    const u32x4 ga = LD(u32x4, lds0 + LDS_GA + t * 160 + c8 * 16);
    const float om = LD(float, lds0 + LDS_OMG + t * 4);
    u32x4 o;
#pragma unroll
    for (int e = 0; e < 4; ++e) {
      const float x0 = e < 2 ? s0[2 * e] : s1[2 * e - 4], x1 = e < 2 ? s0[2 * e + 1] : s1[2 * e - 3];
      o[e] = pk_bf16(bflo(ga[e]) + om * x0, bfhi(ga[e]) + om * x1);
    }
    *(u32x4*)(P.out + (size_t)(64 * m64 + t) * D + 80 * n80 + 8 * c8) = o;
  }
  STAMP(15);
}

extern "C" void kernel_launch(void* const* d_in, const int* in_sizes, int n_in, void* d_out, int out_size,
                              void* d_ws, size_t ws_size, hipStream_t stream) {
  Params P;
  P.u = (const u16*)d_in[0]; P.lar = (const u16*)d_in[1]; P.aim = (const u16*)d_in[2]; P.cre = (const u16*)d_in[3];
  P.cim = (const u16*)d_in[4]; P.ldt = (const u16*)d_in[5]; P.dsk = (const u16*)d_in[6]; P.w1 = (const u16*)d_in[8];
  P.b1 = (const u16*)d_in[9]; P.w2 = (const u16*)d_in[10]; P.b2 = (const u16*)d_in[11]; P.gw = (const u16*)d_in[12];
  P.out = (u16*)d_out; P.ws = (char*)d_ws;
  (void)hipFuncSetAttribute((const void*)fused, hipFuncAttributeMaxDynamicSharedMemorySize, LDS_TOTAL);
  fused<<<256, 512, LDS_TOTAL, stream>>>(P);
}
```

```cpp
#include <hip/hip_runtime.h>
#include <hip/hip_bf16.h>

typedef unsigned short u16;
typedef unsigned int u32;
typedef unsigned long long u64;
typedef short s16x4 __attribute__((ext_vector_type(4)));
typedef __bf16 bf16x2 __attribute__((ext_vector_type(2)));
typedef __bf16 bf16x8 __attribute__((ext_vector_type(8)));
typedef _Float16 f16x2 __attribute__((ext_vector_type(2)));
typedef _Float16 f16x8 __attribute__((ext_vector_type(8)));
typedef float f32x2 __attribute__((ext_vector_type(2)));
typedef float f32x4 __attribute__((ext_vector_type(4)));
typedef u32 u32x2 __attribute__((ext_vector_type(2)));
typedef int i32x4 __attribute__((ext_vector_type(4)));
typedef u32 u32x4 __attribute__((ext_vector_type(4)));
typedef __amdgpu_buffer_rsrc_t rsrc_t;
#define LAS __attribute__((address_space(3)))
#define GAS __attribute__((address_space(1)))
#define RLX_AGENT __ATOMIC_RELAXED, __HIP_MEMORY_SCOPE_AGENT
#define RLX_WG __ATOMIC_RELAXED, __HIP_MEMORY_SCOPE_WORKGROUP
#define DEV __device__ __forceinline__
#define HSLEEP 24
#define G2PRIO 1
#define CVPRIO 2
#define KTPRIO 0
#define G2DP 4
#define SB() __builtin_amdgcn_sched_barrier(0)

constexpr int L = 2048, D = 640, NM = 128, F = 1728;
constexpr u32 WS_FLAGK = 2048;
constexpr u32 WS_FLAGS = 4096;
constexpr u32 WS_FLAGH = WS_FLAGS + 2048;
constexpr u32 WS_FLAGY = WS_FLAGH + 2048;
constexpr u32 WS_GATE = 81920;
constexpr u32 WS_KX = 131072;
constexpr u32 WS_UT = WS_KX + 128 * 4096;
constexpr u32 WS_YT = WS_UT + D * L * 2;
constexpr u32 WS_HT = WS_YT + D * L * 2;
constexpr u32 WS_W2T = WS_HT + 32 * 54 * 4096;
constexpr u32 WS_END = WS_W2T + 8 * 54 * 5120;
#define STAMP(k)
constexpr u32 SCR_W = 15616;
constexpr u32 REG_A = 0, REG_B = 65536;
constexpr u32 SLAB = 64 * 336;
constexpr u32 LDS_SIDE = 131072;
constexpr u32 LDS_X1 = REG_B + 24576, LDS_X2 = REG_B + 40960;
constexpr u32 LDS_GW = LDS_SIDE + 20480;
constexpr u32 LDS_B1 = LDS_GW + 2048;
constexpr u32 LDS_PAR = LDS_B1 + 1024;
constexpr u32 LDS_GA = LDS_GW;
constexpr u32 LDS_OMG = LDS_GA + 10240;
constexpr u32 LDS_B2 = LDS_OMG + 256;
constexpr u32 LDS_CTL = LDS_B2 + 320;
constexpr u32 LDS_TOTAL = LDS_CTL + 64;
#define DBG(i)
constexpr u32 CH_KC0 = 0, CH_K = 2576, CH_U = CH_K + 4608, UCP = 4160, CH_KC1 = 2 * SCR_W - 2576;
constexpr u32 LDS_YT = 0;
constexpr u32 CT_PAIR = LDS_CTL, CT_H = LDS_CTL + 16, CT_Y = LDS_CTL + 20, CT_R = LDS_CTL + 24, CT_FH = LDS_CTL + 28, CT_FY = LDS_CTL + 32, CT_FS = LDS_CTL + 36, CT_Q = LDS_CTL + 40, CT_A = LDS_CTL + 48, CT_S = LDS_CTL + 52, CT_FW = LDS_CTL + 56;

struct Params {
  const u16 *u, *lar, *aim, *cre, *cim, *ldt, *dsk, *w1, *b1, *w2, *b2, *gw;
  u16* out;
  char* ws;
};

DEV float bf2f(u32 x) { return __uint_as_float(x << 16); }
DEV float bflo(u32 x) { return __uint_as_float(x << 16); }
DEV float bfhi(u32 x) { return __uint_as_float(x & 0xffff0000u); }
DEV u32 pk_bf16(float a, float b) { f32x2 v = {a, b}; bf16x2 r = __builtin_convertvector(v, bf16x2); return __builtin_bit_cast(u32, r); }
DEV float rbf(float a) { return bflo(pk_bf16(a, a)); }
DEV u32 pk_f16(f32x2 v) { f16x2 r = __builtin_convertvector(v, f16x2); return __builtin_bit_cast(u32, r); }
DEV f32x2 cmul(f32x2 a, f32x2 b) {
  f32x2 t, r;
  asm("v_pk_mul_f32 %0, %1, %2 op_sel:[0,0] op_sel_hi:[0,1]" : "=v"(t) : "v"(a), "v"(b));
  asm("v_pk_fma_f32 %0, %1, %2, %3 op_sel:[1,1,0] op_sel_hi:[1,0,1] neg_lo:[1,0,0]" : "=v"(r) : "v"(a), "v"(b), "v"(t));
  return r;
}
DEV f32x2 cexp_p(float ar, float ai, float p) {
  float e = __builtin_amdgcn_exp2f(ar * p * 1.44269504088896341f);
  float t = ai * p;
  float hi = t * 0.15915494309189535f;
  float lo = __builtin_fmaf(t, 0.15915494309189535f, -hi);
  lo = __builtin_fmaf(t, 6.4206383e-9f, lo);
  float fr = (hi - __builtin_rintf(hi)) + lo;
  return (f32x2){e * __builtin_amdgcn_cosf(fr), e * __builtin_amdgcn_sinf(fr)};
}
#define WAIT_VM(n) asm volatile("s_waitcnt vmcnt(" #n ")" ::: "memory")
#define WAIT_LGKM0() asm volatile("s_waitcnt lgkmcnt(0)" ::: "memory")
DEV void bar() { asm volatile("s_waitcnt lgkmcnt(0)" ::: "memory"); __builtin_amdgcn_s_barrier(); asm volatile("" ::: "memory"); }
DEV void rawbar() { asm volatile("" ::: "memory"); __builtin_amdgcn_s_barrier(); asm volatile("" ::: "memory"); }

DEV void glds1(const void* base, u32 voff, u32 lds) {
  u32 keep;
  asm volatile("s_mov_b32 %0, m0\n\ts_mov_b32 m0, %3\n\ts_nop 0\n\tglobal_load_lds_dwordx4 %1, %2\n\ts_mov_b32 m0, %0"
               : "=&s"(keep) : "v"(voff), "s"(base), "s"(lds) : "memory");
}
DEV void glds1p(const void* p, u32 lds) {
  u32 keep;
  asm volatile("s_mov_b32 %0, m0\n\ts_mov_b32 m0, %2\n\ts_nop 0\n\tglobal_load_lds_dwordx4 %1, off\n\ts_mov_b32 m0, %0"
               : "=&s"(keep) : "v"(p), "s"(lds) : "memory");
}
DEV void glds4(rsrc_t rs, u32 v0, u32 v1, u32 v2, u32 v3, u32 s, u32 step, u32 lds) {
  u32 keep;
  asm volatile("s_mov_b32 %0, m0\n\ts_mov_b32 m0, %10\n\ts_nop 0\n\t"
               "buffer_load_dwordx4 %1, %5, %6 offen lds\n\t"
               "buffer_load_dwordx4 %2, %5, %7 offen offset:1024 lds\n\t"
               "buffer_load_dwordx4 %3, %5, %8 offen offset:2048 lds\n\t"
               "buffer_load_dwordx4 %4, %5, %9 offen offset:3072 lds\n\t"
               "s_mov_b32 m0, %0"
               : "=&s"(keep) : "v"(v0), "v"(v1), "v"(v2), "v"(v3), "s"(rs), "s"(s), "s"(s + step),
                 "s"(s + 2 * step), "s"(s + 3 * step), "s"(lds) : "memory");
}
DEV u32x2 tr_read(u32 a) {
  s16x4 r = __builtin_amdgcn_ds_read_tr16_b64_v4i16((LAS s16x4*)(size_t)a);
  return __builtin_bit_cast(u32x2, r);
}
DEV bf16x8 mk8(u32x2 a, u32x2 b) { u32x4 v = {a.x, a.y, b.x, b.y}; return __builtin_bit_cast(bf16x8, v); }
DEV f32x4 mma(bf16x8 a, bf16x8 b, f32x4 c) { return __builtin_amdgcn_mfma_f32_16x16x32_bf16(a, b, c, 0, 0, 0); }
#define LD(T, a) (*(const LAS T*)(size_t)(a))
#define ST(T, a) (*(LAS T*)(size_t)(a))

template <int SLEEP>
DEV void poll_flags(const char* p, u64 tag) {
#pragma unroll 1
  for (u32 spins = 0; spins < (1u << 18); ++spins) {
    u64 v = __hip_atomic_load((const GAS u64*)p, RLX_AGENT);
    if (__all(v == tag)) break;
    __builtin_amdgcn_s_sleep(SLEEP);
  }
  __builtin_amdgcn_fence(__ATOMIC_ACQUIRE, "wavefront");
  asm volatile("" ::: "memory");
}
DEV void put_flag(char* p, u64 tag) { __hip_atomic_store((GAS u64*)p, tag, RLX_AGENT); }
DEV u32 lds_arrive(u32 a, int lane) {
  WAIT_LGKM0();
  u32 r = 0;
  if (lane == 0) r = __hip_atomic_fetch_add((LAS u32*)(size_t)a, 1u, RLX_WG);
  return __builtin_amdgcn_readfirstlane(r);
}
DEV void lds_wait(u32 a, u32 target) {
#pragma unroll 1
  for (u32 spins = 0; spins < (1u << 22); ++spins) {
    u32 v = __builtin_amdgcn_readfirstlane(__hip_atomic_load((LAS u32*)(size_t)a, RLX_WG));
    if (v >= target) break;
    __builtin_amdgcn_s_sleep(1);
  }
  asm volatile("" ::: "memory");
}

DEV void kgen(u32 pp, float ldt, u32 (&pk)[57], float& kscale) {
  const float dt = rbf(expf(ldt));
  const float ar = -rbf(expf(bf2f(LD(u16, pp)))) * dt, ai = bf2f(LD(u16, pp + 128)) * dt;
  const float cr = 2.f * bf2f(LD(u16, pp + 256)) * dt, ci = 2.f * bf2f(LD(u16, pp + 384)) * dt;
  u32 mx = max(__float_as_uint(cr) & 0x7fffffffu, __float_as_uint(ci) & 0x7fffffffu);
  mx = max(mx, (u32)__builtin_amdgcn_update_dpp(0, mx, 0x128, 0xf, 0xf, false));
  mx = max(mx, (u32)__builtin_amdgcn_update_dpp(0, mx, 0x124, 0xf, 0xf, false));
  mx = max(mx, (u32)__builtin_amdgcn_update_dpp(0, mx, 0x122, 0xf, 0xf, false));
  mx = max(mx, (u32)__builtin_amdgcn_update_dpp(0, mx, 0x121, 0xf, 0xf, false));
  u32 eb = max(max((u32)__builtin_amdgcn_readlane(mx, 0), (u32)__builtin_amdgcn_readlane(mx, 16)),
               max((u32)__builtin_amdgcn_readlane(mx, 32), (u32)__builtin_amdgcn_readlane(mx, 48))) >> 23;
  eb = eb < 40u ? 40u : (eb > 230u ? 230u : eb);
  const float inv = __uint_as_float((127u + 127u + 12u - eb) << 23);
  kscale = __uint_as_float((eb - 12u - 14u) << 23);
  const f32x2 e1 = cexp_p(ar, ai, 1.f), e8 = cexp_p(ar, -ai, 8.f), e128 = cexp_p(ar, ai, 128.f);
  {
    f32x2 G[16];
    const f32x2 e16 = cmul(e8, e8), e32 = cmul(e16, e16), e64 = cmul(e32, e32);
    G[0] = (f32x2){cr * inv, -ci * inv};
    G[1] = cmul(G[0], e8);
#pragma unroll
    for (int j = 0; j < 2; ++j) G[2 + j] = cmul(G[j], e16);
#pragma unroll
    for (int j = 0; j < 4; ++j) G[4 + j] = cmul(G[j], e32);
#pragma unroll
    for (int j = 0; j < 8; ++j) G[8 + j] = cmul(G[j], e64);
#pragma unroll
    for (int j = 0; j < 16; ++j) pk[j] = pk_f16(G[j]);
  }
  f32x2 Pw[8], Q[17];
  const f32x2 e2 = cmul(e1, e1), e4 = cmul(e2, e2);
  Pw[1] = e1; Pw[2] = e2; Pw[3] = cmul(e1, e2); Pw[4] = e4; Pw[5] = cmul(e1, e4); Pw[6] = cmul(e2, e4); Pw[7] = cmul(Pw[3], e4);
  const f32x2 e256 = cmul(e128, e128), e512 = cmul(e256, e256), e1024 = cmul(e512, e512);
  Q[1] = e128 * 16384.f; Q[2] = e256 * 16384.f; Q[4] = e512 * 16384.f; Q[8] = e1024 * 16384.f;
  Q[3] = cmul(Q[1], e256);
  Q[5] = cmul(Q[1], e512); Q[6] = cmul(Q[2], e512); Q[7] = cmul(Q[3], e512);
#pragma unroll
  for (int j = 1; j < 9; ++j) Q[8 + j] = cmul(Q[j], e1024);
  pk[16] = 0x00007400u;
#pragma unroll
  for (int j = 1; j < 8; ++j) pk[16 + j] = pk_f16(Pw[j] * 16384.f);
  pk[24] = pk_f16(Q[1]);
#pragma unroll
  for (int j = 1; j < 8; ++j) pk[24 + j] = pk_f16(cmul(Q[1], Pw[j]));
#pragma unroll
  for (int i = 2; i < 4; ++i) {
    pk[32 + 4 * (i - 2)] = pk_f16(Q[i]);
#pragma unroll
    for (int j = 1; j < 4; ++j) pk[32 + 4 * (i - 2) + j] = pk_f16(cmul(Q[i], Pw[2 * j]));
  }
#pragma unroll
  for (int i = 4; i < 8; ++i) { pk[40 + 2 * (i - 4)] = pk_f16(Q[i]); pk[41 + 2 * (i - 4)] = pk_f16(cmul(Q[i], Pw[4])); }
#pragma unroll
  for (int i = 8; i < 17; ++i) pk[48 + i - 8] = pk_f16(Q[i]);
}
DEV void ktable(u32 scr, u32 kb, const u32 (&pk)[57], float kscale, int lane) {
  const int l15 = lane & 15, q4 = lane >> 4;
#pragma unroll
  for (int j = 0; j < 57; ++j) ST(u32, scr + lane * 4 + j * 272) = pk[j];
  asm volatile("" ::: "memory");
  {
    f32x4 kc[3];
#pragma unroll
    for (int T = 0; T < 3; ++T) kc[T] = (f32x4){0.f, 0.f, 0.f, 0.f};
    const u32 rp = scr + l15 * 272 + q4 * 16;
    const u32 rp2 = scr + (48 + (l15 > 8 ? 8 : l15)) * 272 + q4 * 16;
    f16x8 fr[4][4];
#pragma unroll
    for (int ks = 0; ks < 4; ++ks) {
      fr[ks][0] = LD(f16x8, rp + ks * 64); fr[ks][1] = LD(f16x8, rp + 16 * 272 + ks * 64);
      fr[ks][2] = LD(f16x8, rp + 32 * 272 + ks * 64); fr[ks][3] = LD(f16x8, rp2 + ks * 64);
    }
    SB();
#pragma unroll
    for (int ks = 0; ks < 4; ++ks)
#pragma unroll
      for (int T = 0; T < 3; ++T) kc[T] = __builtin_amdgcn_mfma_f32_16x16x32_f16(fr[ks][1 + T], fr[ks][0], kc[T], 0, 0, 0);
    WAIT_LGKM0();
    ST(f32x4, kb + (128 * (q4 >> 1) + 8 * l15 + 4 * (q4 & 1)) * 4) = kc[0] * kscale;
    const f32x4 v1 = kc[1] * kscale, v2 = kc[2] * kscale;
    if (q4 < 2) {
      ST(f32x4, kb + (256 + 64 * q4 + 4 * l15) * 4) = v1;
#pragma unroll
      for (int rr = 0; rr < 4; ++rr) ST(float, kb + (512 + 16 * (4 * q4 + rr) + l15) * 4) = v2[rr];
    } else {
      ST(f32x2, kb + (384 + 64 * (q4 - 2) + 2 * l15) * 4) = (f32x2){v1[0], v1[1]};
      ST(f32x2, kb + (416 + 64 * (q4 - 2) + 2 * l15) * 4) = (f32x2){v1[2], v1[3]};
      if (lane == 32) ST(float, kb + 640 * 4) = v2[0];
    }
  }
}
DEV void expand(u32 chb, int half, int lane, u32x4& c0, u32x4& c1) {
  {
    auto tab = [&](int i) { return LD(float, chb + CH_KC0 + i * 4) + LD(float, chb + CH_KC1 + i * 4); };
    const int m = 255 - 64 * half - lane;
    const float a = tab(512 + m - 127), b = tab(512 + m - 128), c = (m & 1) ? a : b;
    const u32 bb = pk_bf16(b, b);
    c0 = (u32x4){pk_bf16(a, a), pk_bf16(a, c), bb, bb};
    if (half == 0) {
      const int i = 384 + 2 * (63 - lane);
      const float x = tab(i + 2), y = tab(i + 1), z = tab(i);
      c1 = (u32x4){pk_bf16(x, x), pk_bf16(y, y), pk_bf16(y, z), pk_bf16(z, z)};
    } else {
      const bool lo = lane >= 32;
      const int i = lo ? 8 * (63 - lane) : 256 + 4 * (31 - lane);
      const f32x4 s0 = LD(f32x4, chb + CH_KC0 + i * 4) + LD(f32x4, chb + CH_KC1 + i * 4);
      const f32x4 s1 = LD(f32x4, chb + CH_KC0 + i * 4 + 16) + LD(f32x4, chb + CH_KC1 + i * 4 + 16);
      c1 = (u32x4){pk_bf16(lo ? s1[3] : s1[0], lo ? s1[2] : s0[3]), pk_bf16(lo ? s1[1] : s0[2], lo ? s1[0] : s0[2]),
                   pk_bf16(lo ? s0[3] : s0[2], lo ? s0[2] : s0[1]), pk_bf16(lo ? s0[1] : s0[0], s0[0])};
    }
  }
}

struct ConvFrags { bf16x8 A[2][8], B[2][8]; };
template <bool NEXT>
DEV void conv_step(ConvFrags& f, int set, u32 pa, u32 pa8, u32 pb, f32x4 (&acc)[8]) {
  u32x2 lo[8], hi[8];
#pragma unroll
  for (int g = 0; g < 12; ++g) {
    if (NEXT) {
#pragma unroll
      for (int d = 2 * g; d < 2 * g + 2; ++d) {
        const int k = d / 3;
        if (d % 3 == 0) lo[k] = LD(u32x2, pa + 512 * k);
        if (d % 3 == 1) { hi[k] = LD(u32x2, pa8 + 512 * k); f.A[set ^ 1][k] = mk8(lo[k], hi[k]); }
        if (d % 3 == 2) f.B[set ^ 1][7 - k] = LD(bf16x8, pb + 512 * k);
      }
    }
#pragma unroll
    for (int j = 3 * g; j < 3 * g + 3; ++j) {
      const int k = j < 1 ? 0 : j < 3 ? 1 : j < 6 ? 2 : j < 10 ? 3 : j < 15 ? 4 : j < 21 ? 5 : j < 28 ? 6 : 7, e = j - k * (k + 1) / 2;
      acc[e + 7 - k] = mma(f.A[set][e], f.B[set][7 - k], acc[e + 7 - k]);
    }
    if (NEXT) {
#pragma unroll
      for (int j = 0; j < 2; ++j) { __builtin_amdgcn_sched_group_barrier(0x8, 1, 0); __builtin_amdgcn_sched_group_barrier(0x100, 1, 0); }
      __builtin_amdgcn_sched_group_barrier(0x8, 1, 0);
    }
    SB();
  }
}

__global__ __launch_bounds__(512, 2) void fused(Params P_) {
  extern __shared__ __attribute__((aligned(16))) char smem[];
  u64 tz0_;
  const __attribute__((address_space(4))) Params* kp_ = (const __attribute__((address_space(4))) Params*)__builtin_amdgcn_kernarg_segment_ptr();
  asm volatile("s_memrealtime %0\n\ts_waitcnt lgkmcnt(0)" : "=s"(tz0_), "+s"(kp_) :: "memory");
#if __HIP_DEVICE_COMPILE__
  const Params P = *kp_;
#else
  const Params P = P_;
#endif
  const int tid = threadIdx.x, lane = tid & 63;
  const int wave = __builtin_amdgcn_readfirstlane(tid >> 6);
  const int bid = blockIdx.x, xcd = bid & 7, r = bid >> 3;
  const bool regular = r < 28;
  const int quad = (wave >> 2) ^ 1, qw = wave & 3, half = qw & 1;
  const int l15 = lane & 15, q4 = lane >> 4;
  const u32 lds0 = (u32)(size_t)(LAS char*)smem;
  char* ws = P.ws;
#define PADTO(t) do { while (__builtin_amdgcn_s_memrealtime() - tz0_ < (t)) __builtin_amdgcn_s_sleep(1); } while (0)
  const int rq = (r * 37) >> 8, nl = r - 7 * rq, mi = (xcd >> 1) * 4 + rq, ni = (xcd & 1) * 7 + nl;
  const int rot = nl + 7 * rq - 10 * ((nl + 7 * rq) / 10);
  const int m64 = (xcd >> 1) * 8 + (r >> 2), n80 = (xcd & 1) * 4 + (r & 3);
  const rsrc_t wsr = __builtin_amdgcn_make_buffer_rsrc(ws, 0, WS_END, 0x00020000);
  const int chan0 = 80 * xcd + 2 * r;
  const u32 chb = lds0 + REG_A + (qw >> 1) * (2 * SCR_W);
  const int ch = chan0 + (qw >> 1);
  const bool hasx = !(r & 1);
  u64 tag;

  auto par_load = [&](int ch, u32 dst) {
    const int a = (lane >> 3) & 3;
    const u16 *p0 = P.lar, *p1 = P.aim, *p2 = P.cre, *p3 = P.cim;
    asm volatile("" : "+s"(p0), "+s"(p1), "+s"(p2), "+s"(p3));
    const u16* p = a == 0 ? p0 : a == 1 ? p1 : a == 2 ? p2 : p3;
    if (lane < 32) glds1p(p + ch * NM + 64 * half + (lane & 7) * 8, dst);
  };
  const bool isw = qw >= 2, side = !isw || m64 < 27;
  auto side_load = [&]() {
#pragma unroll
    for (int i = 0; i < 5; ++i) {
      const int o = i * 64 + lane, row = (o * 6554) >> 16, c = o - 10 * row;
      glds1(isw ? P.w2 : P.u, (u32)((m64 * 64 + half * 32 + row) * (D * 2) + n80 * 160 + c * 16), lds0 + LDS_SIDE + qw * 5120 + i * 1024);
    }
  };
  auto side_store = [&]() {
    const u32 p = lds0 + LDS_SIDE + qw * 5120 + ((isw ? 4 : 8) * q4 + (l15 >> 2)) * 160 + 8 * (lane & 3), p2 = p + (isw ? 16 * 160 : 4 * 160);
    const u32 o = isw ? WS_W2T + (u32)((n80 * 54 + 2 * m64 + half) * 5120) + lane * 16
                      : WS_UT + (u32)((80 * n80 + l15) * (L * 2) + (64 * m64 + 32 * half) * 2 + q4 * 16);
    u32x2 a[5], b[5];
#pragma unroll
    for (int i = 0; i < 5; ++i) { a[i] = tr_read(p + 32 * i); b[i] = tr_read(p2 + 32 * i); }
#pragma unroll
    for (int i = 0; i < 5; ++i)
      __builtin_amdgcn_raw_buffer_store_b128((u32x4){a[i].x, a[i].y, b[i].x, b[i].y}, wsr, o + i * (isw ? 1024 : 16 * L * 2), 0, 16);
  };

  u32 ldt2 = 0;
  if (!regular) {
    const int xi = 4 * (r - 28) + 2 * quad + (qw >> 1);
    par_load(80 * xcd + 64 + xi, lds0 + LDS_PAR + (4 + wave) * 512);
    if (quad == 0) par_load(chan0 + (qw >> 1), lds0 + LDS_PAR + qw * 512);
    if (quad == 1 && side) side_load();
    if (tid < 16) ST(u32, lds0 + LDS_CTL + tid * 4) = 0;
    u32 ldx;
    asm volatile("s_load_dwordx2 %0, %3, 0x0\n\ts_load_dword %1, %4, 0x0\n\ts_load_dword %2, %5, 0x0\n\ts_waitcnt lgkmcnt(0)"
                 : "=&s"(tag), "=&s"(ldt2), "=&s"(ldx) : "s"(ws), "s"(P.ldt + chan0), "s"(P.ldt + 80 * xcd + 64 + (xi & ~1)) : "memory");
    tag += 1;
    if (quad == 1 && side) WAIT_VM(5); else WAIT_VM(0);
    u32 pk[57];
    float kscale;
    kgen(lds0 + LDS_PAR + (4 + wave) * 512 + lane * 2, (xi & 1) ? bfhi(ldx) : bflo(ldx), pk, kscale);
#pragma unroll
    for (int j = 0; j < 57; ++j) asm volatile("" : "+v"(pk[j]));
    WAIT_VM(0);
    STAMP(1);
    if (quad == 1 && side) side_store();
    const u32 xb = chb + (quad ? REG_B : 0);
    ktable(xb + half * SCR_W, xb + (half ? CH_KC1 : CH_KC0), pk, kscale, lane);
    bar();
    u32x4 c0, c1;
    expand(xb, half, lane, c0, c1);
    const u32 ko = WS_KX + (u32)((xcd * 16 + xi) * 4096 + half * 1024) + lane * 16;
    __builtin_amdgcn_raw_buffer_store_b128(c0, wsr, ko, 0, 16);
    __builtin_amdgcn_raw_buffer_store_b128(c1, wsr, ko + 2048, 0, 16);
    WAIT_VM(0);
    bar();
    if (tid == 0) { put_flag(ws + WS_FLAGK + (xcd * 4 + r - 28) * 8, tag); put_flag(ws + WS_FLAGS + (n80 * 32 + m64) * 8, tag); }
    STAMP(2);
  }

  if (quad == 0) {
    const rsrc_t xr = __builtin_amdgcn_make_buffer_rsrc((void*)P.u, 0, L * D * 2, 0x00020000);
    const rsrc_t wr = __builtin_amdgcn_make_buffer_rsrc((void*)P.w1, 0, D * F * 2, 0x00020000);
    const u32 xv = (u32)((lane >> 3) * (D * 2) + ((lane & 7) ^ q4) * 16);
    const u32 wv = (u32)(q4 * (F * 2) + (l15 ^ (q4 << 2)) * 16);
    const u32 xs = (u32)((mi * 128 + 32 * qw) * (D * 2)), wso = (u32)(16 * qw * (F * 2) + ni * 256);
    auto stage = [&](int i) {
      const u32 dst = lds0 + ((i + 2) & 3) * 32768 + qw * 4096;
      const int k = i + rot >= 10 ? i + rot - 10 : i + rot;
      glds4(xr, xv, xv ^ 64, xv, xv ^ 64, xs + k * 128, 8 * D * 2 - 1024, dst);
      glds4(wr, wv, wv ^ 16, wv ^ 32, wv ^ 48, wso + k * (64 * F * 2), 4 * F * 2 - 1024, dst + 16384);
    };
    if (regular) {
      __builtin_amdgcn_s_setprio(3);
      rawbar();
      par_load(chan0 + (qw >> 1), lds0 + LDS_PAR + qw * 512);
      stage(0);
      if (qw == 0) glds1(P.b1, (u32)(ni * 256 + (lane & (ni == 13 ? 7 : 15)) * 16), lds0 + LDS_B1);
      if (qw == 1 && ni == 13) { glds1(P.gw, lane * 16, lds0 + LDS_GW); glds1(P.gw, 1024 + l15 * 16, lds0 + LDS_GW + 1024); }
      stage(1);
      asm volatile("s_load_dwordx2 %0, %2, 0x0\n\ts_load_dword %1, %3, 0x0\n\ts_waitcnt lgkmcnt(0)"
                   : "=&s"(tag), "=&s"(ldt2) : "s"(ws), "s"(P.ldt + chan0) : "memory");
      tag += 1;
      WAIT_VM(16);
    }
    u32 pk[57];
    float kscale;
    kgen(lds0 + LDS_PAR + qw * 512 + lane * 2, (qw >> 1) ? bfhi(ldt2) : bflo(ldt2), pk, kscale);
#pragma unroll
    for (int j = 0; j < 57; ++j) asm volatile("" : "+v"(pk[j]));
    STAMP(5);
    if (regular) {
      stage(2);
#pragma unroll 1
      for (int s = 0; s < 7; ++s) {
        WAIT_VM(8);
        rawbar();
        stage(s + 3);
        if (s == 3 && tid == 256) put_flag(ws + WS_FLAGS + (n80 * 32 + m64) * 8, tag);
      }
      WAIT_VM(8);
      rawbar();
      WAIT_VM(0);
      rawbar();
    }
    STAMP(4);
    __builtin_amdgcn_s_setprio(KTPRIO);
    if (qw == 0) {
      if (!regular) __builtin_amdgcn_s_sleep(HSLEEP);
      poll_flags<12>(lane < 32 ? ws + WS_FLAGS + (xcd * 32 + lane) * 8 : lane < 59 ? ws + WS_FLAGS + (n80 * 32 + lane - 32) * 8
                                                                                   : ws + WS_FLAGK + (xcd * 4 + (r >> 3)) * 8, tag);
      if (lane == 0) __hip_atomic_store((LAS u32*)(size_t)(lds0 + CT_FS), 1u, RLX_WG);
    } else {
      lds_wait(lds0 + CT_FS, 1u);
    }
    {
      u32x4 ud[3], kx;
      u32x2 un[3];
      const u32 uo = WS_UT + (u32)(ch * (L * 2) + half * 2048);
#pragma unroll
      for (int i = 0; i < 2; ++i) {
        ud[i] = __builtin_amdgcn_raw_buffer_load_b128(wsr, lane * 16 + i * 1024, uo, 16);
        un[i] = __builtin_amdgcn_raw_buffer_load_b64(wsr, lane * 16 + i * 1024 + 16, uo, 16);
      }
      const u32 xo = WS_UT + (u32)((80 * xcd + 64 + (r >> 1)) * (L * 2) + qw * 1024);
      ud[2] = __builtin_amdgcn_raw_buffer_load_b128(wsr, lane * 16, xo, 16);
      un[2] = __builtin_amdgcn_raw_buffer_load_b64(wsr, lane * 16 + 16, xo, 16);
      kx = __builtin_amdgcn_raw_buffer_load_b128(wsr, lane * 16, WS_KX + (u32)((xcd * 16 + (r >> 1)) * 4096 + qw * 1024), 16);
      STAMP(1);
      ktable(chb + half * SCR_W, chb + (half ? CH_KC1 : CH_KC0), pk, kscale, lane);
      lds_arrive(lds0 + CT_PAIR + (qw >> 1) * 4, lane);
      lds_wait(lds0 + CT_PAIR + (qw >> 1) * 4, 2);
      u32x4 c0, c1;
      expand(chb, half, lane, c0, c1);
      ST(u32x4, chb + CH_K + (64 * half + lane) * 16) = c0;
      ST(u32x4, chb + CH_K + (128 + 64 * half + lane) * 16) = c1;
      auto put4 = [&](u32 a, u32x4 d, u32x2 n) {
        const u32 s0 = __builtin_amdgcn_alignbit(d[1], d[0], 16), s1 = __builtin_amdgcn_alignbit(d[2], d[1], 16),
                  s2 = __builtin_amdgcn_alignbit(d[3], d[2], 16), s3 = __builtin_amdgcn_alignbit(n[0], d[3], 16),
                  s4 = __builtin_amdgcn_alignbit(n[1], n[0], 16);
        ST(u32x4, a) = d;
        ST(u32x4, a + UCP) = (u32x4){s0, s1, s2, s3};
        ST(u32x4, a + 2 * UCP) = (u32x4){d[1], d[2], d[3], n[0]};
        ST(u32x4, a + 3 * UCP) = (u32x4){s1, s2, s3, s4};
      };
      auto edges = [&](u32 blk, u32x4 d) {
        const u32x4 z = {0, 0, 0, 0};
        if (lane < 2) put4(blk + CH_U + 16 - lane * 16, z, lane ? (u32x2){0, 0} : (u32x2){d[0], d[1]});
        else if (lane < 32) ST(u32x4, blk + CH_K + (254 + lane) * 16) = z;
      };
#pragma unroll
      for (int i = 0; i < 2; ++i) put4(chb + CH_U + 32 + half * 2048 + i * 1024 + lane * 16, ud[i], un[i]);
      if (half == 0) edges(chb, ud[0]);
      if (hasx) {
        if (regular) lds_wait(lds0 + CT_R, 4u);
        put4(lds0 + REG_B + CH_U + 32 + qw * 1024 + lane * 16, ud[2], un[2]);
        ST(u32x4, lds0 + REG_B + CH_K + qw * 1024 + lane * 16) = kx;
        if (qw == 0) edges(lds0 + REG_B, ud[2]);
      }
    }
    lds_arrive(lds0 + CT_Q, lane);
    STAMP(8);
    __builtin_amdgcn_s_setprio(G2PRIO);
    if (regular) lds_wait(lds0 + CT_R, 4u);
    constexpr int NS = 14, DP = G2DP;
    const int first = qw >> 1;
    const int k0 = (qw < 2 ? 14 * qw : 28 + 13 * (qw - 2)) - first;
    const u32 hb = WS_HT + (u32)((m64 * 54 + k0) * 4096), wb = WS_W2T + (u32)((n80 * 54 + k0) * 5120);
    const u32 lo = lane * 16;
    u32x4 fr[DP][9];
    auto load1 = [&](int s, int i) {
      const int j = s;
      fr[s % DP][i] = i < 5 ? __builtin_amdgcn_raw_buffer_load_b128(wsr, lo + (i & 3) * 1024, wb + j * 5120 + (i >> 2) * 4096, 16)
                            : __builtin_amdgcn_raw_buffer_load_b128(wsr, lo + (i - 5) * 1024, hb + j * 4096, 16);
    };
    if (first == 0) {
#pragma unroll
      for (int i = 0; i < 5; ++i) load1(0, i);
    }
#pragma unroll
    for (int i = 0; i < 5 * (DP - 2); ++i) load1(1 + i / 5, i % 5);
    const u32 tch = __builtin_amdgcn_raw_buffer_load_b32(wsr, (u32)(((m64 & 7) * 270 + qw * 64 + lane) * 128), WS_W2T + (u32)(n80 * 54 * 5120), 16);
    STAMP(6);
    if (qw == 0) {
      if (regular) lds_wait(lds0 + CT_H, 4u);
      poll_flags<4>(ws + WS_FLAGH + ((m64 >> 1) * 16 + (lane < 14 ? lane : 0)) * 8, tag);
      if (lane == 0) __hip_atomic_store((LAS u32*)(size_t)(lds0 + CT_FH), 1u, RLX_WG);
    } else {
      lds_wait(lds0 + CT_FH, 1u);
    }
    asm volatile("" :: "v"(tch));
    STAMP(11);
    if (first == 0) {
#pragma unroll
      for (int i = 5; i < 9; ++i) load1(0, i);
    }
#pragma unroll
    for (int i = 0; i < 4 * (DP - 2); ++i) load1(1 + i / 4, 5 + i % 4);
    f32x4 acc[5][4];
#pragma unroll
    for (int a = 0; a < 5; ++a)
#pragma unroll
      for (int b = 0; b < 4; ++b) acc[a][b] = (f32x4){0.f, 0.f, 0.f, 0.f};
    SB();
#pragma unroll
    for (int s = 0; s < NS; ++s) {
      if (s > 0 || first == 0) {
#pragma unroll
        for (int a = 0; a < 5; ++a) {
          if (s + DP - 1 < NS) { load1(s + DP - 1, 2 * a); if (a < 4) load1(s + DP - 1, 2 * a + 1); }
#pragma unroll
          for (int b = 0; b < 4; ++b)
            acc[a][b] = mma(__builtin_bit_cast(bf16x8, fr[s % DP][a]), __builtin_bit_cast(bf16x8, fr[s % DP][5 + b]), acc[a][b]);
          SB();
        }
      } else {
#pragma unroll
        for (int i = 0; i < 9; ++i) load1(DP - 1, i);
        SB();
      }
    }
    STAMP(12);
    lds_wait(lds0 + CT_Y, 4u);
    const u32 sl = lds0 + REG_B + qw * SLAB + l15 * 336 + q4 * 16;
#pragma unroll
    for (int a = 0; a < 5; ++a)
#pragma unroll
      for (int b = 0; b < 4; ++b) ST(f32x4, sl + b * 16 * 336 + a * 64) = acc[a][b];
  } else {
    const bool lastn = ni == 13;
    if (regular) {
      if (side) side_load();
      rawbar();
      if (tid < 16) ST(u32, lds0 + LDS_CTL + tid * 4) = 0;
      asm volatile("s_load_dwordx2 %0, %1, 0x0\n\ts_waitcnt lgkmcnt(0)" : "=&s"(tag) : "s"(ws) : "memory");
      tag += 1;
      WAIT_VM(0);
      STAMP(5);
      if (side) side_store();
      STAMP(6);
      __builtin_amdgcn_s_setprio(2);
      const int wf = qw & 1, wt = qw >> 1;
      u32 xa[2], wa[4][2];
      xa[0] = lds0 + 65536 + (u32)((64 * wt + l15) * 128 + ((q4 ^ (l15 >> 1)) * 16)); xa[1] = xa[0] ^ 64;
      f32x4 acc[4][4];
      if (!(lastn && wf)) {
#pragma unroll
        for (int t = 0; t < 2; ++t) {
          const int qq = l15 >> 2, p = lane & 3;
          const int row = 8 * q4 + 4 * t + qq, c = 8 * wf + (p >> 1);
          const u32 a0 = lds0 + 65536 + 16384u + 256u * row + 16u * (c ^ ((qq << 2) | ((2 * q4 + t) & 3))) + 8u * (p & 1);
#pragma unroll
          for (int ft = 0; ft < 4; ++ft) wa[ft][t] = a0 ^ (ft << 5);
        }
        bf16x8 A[2][4], B[2][4];
        auto rdf = [&](int set, int i, u32 so, int kk) {
          A[set][i] = mk8(tr_read(wa[i][0] + so + kk * 8192), tr_read(wa[i][1] + so + kk * 8192));
          B[set][i] = LD(bf16x8, xa[kk] + so + i * 2048);
        };
        auto hstep = [&](int set, u32 so, int kk) {
          constexpr int ord[16][2] = {{0, 0}, {0, 1}, {1, 0}, {1, 1}, {0, 2}, {1, 2}, {2, 0}, {2, 1},
                                      {2, 2}, {0, 3}, {1, 3}, {2, 3}, {3, 0}, {3, 1}, {3, 2}, {3, 3}};
#pragma unroll
          for (int g = 0; g < 4; ++g) {
            rdf(set ^ 1, g, so, kk);
#pragma unroll
            for (int j = 0; j < 4; ++j) {
              const int a = ord[4 * g + j][0], b = ord[4 * g + j][1];
              acc[a][b] = mma(A[set][a], B[set][b], acc[a][b]);
            }
#pragma unroll
            for (int j = 0; j < 3; ++j) { __builtin_amdgcn_sched_group_barrier(0x8, 1, 0); __builtin_amdgcn_sched_group_barrier(0x100, 1, 0); }
            __builtin_amdgcn_sched_group_barrier(0x8, 1, 0);
            SB();
          }
        };
        bar();
        STAMP(1);
#pragma unroll
        for (int i = 0; i < 4; ++i) rdf(0, i, 0, 0);
#pragma unroll
        for (int ft = 0; ft < 4; ++ft) {
          const u32x2 bi = LD(u32x2, lds0 + LDS_B1 + (wf * 64 + ft * 16 + 4 * q4) * 2);
#pragma unroll
          for (int tt = 0; tt < 4; ++tt) acc[ft][tt] = (f32x4){bflo(bi.x), bfhi(bi.x), bflo(bi.y), bfhi(bi.y)};
        }
#pragma unroll 1
        for (int p = 0; p < 5; ++p) {
          DBG(4 * p); if (p > 0) rawbar(); DBG(4 * p + 1);
          SB();
          hstep(0, 0, 1); hstep(1, 32768, 0);
          if (p == 1) WAIT_VM(0);
          if (p < 4) rawbar();
          DBG(4 * p + 3);
          SB();
          hstep(0, 32768, 1);
          xa[0] ^= 65536; xa[1] ^= 65536;
#pragma unroll
          for (int ft = 0; ft < 4; ++ft) { wa[ft][0] ^= 65536; wa[ft][1] ^= 65536; }
          SB();
          hstep(1, 0, 0);
        }
        STAMP(2);
        const u32 ho = WS_HT + (u32)(((2 * mi + wt) * 54 + 4 * ni + 2 * wf) * 4096);
#pragma unroll
        for (int kh = 0; kh < 2; ++kh)
#pragma unroll
          for (int tt = 0; tt < 4; ++tt) {
            u32x4 o;
#pragma unroll
            for (int h = 0; h < 2; ++h) {
              f32x4 v = __builtin_bit_cast(f32x4, __builtin_elementwise_max(__builtin_bit_cast(i32x4, acc[2 * kh + h][tt]), (i32x4){0, 0, 0, 0}));
              v = v * v;
              o[2 * h] = pk_bf16(v[0], v[1]); o[2 * h + 1] = pk_bf16(v[2], v[3]);
            }
            __builtin_amdgcn_raw_buffer_store_b128(o, wsr, ho + kh * 4096 + lane * 16 + tt * 1024, 0, 16);
          }
      } else {
#pragma unroll
        for (int tt = 0; tt < 4; ++tt) acc[0][tt] = (f32x4){0.f, 0.f, 0.f, 0.f};
#pragma unroll 1
        for (int s = 0; s < 10; ++s) {
          if (s == 3) WAIT_VM(0);
          if (s < 9) bar();
          const int k = s + rot >= 10 ? s + rot - 10 : s + rot;
#pragma unroll
          for (int kk = 0; kk < 2; ++kk) {
            const bf16x8 Ag = LD(bf16x8, lds0 + LDS_GW + k * 128 + kk * 64 + q4 * 16);
#pragma unroll
            for (int tt = 0; tt < 4; ++tt) acc[0][tt] = mma(Ag, LD(bf16x8, (xa[kk] ^ (((s + 2) & 3) * 32768) ^ 65536) + tt * 2048), acc[0][tt]);
          }
        }
        float pre = q4 == 0 ? acc[0][0][0] : q4 == 1 ? acc[0][1][0] : q4 == 2 ? acc[0][2][0] : acc[0][3][0];
        pre = rbf(pre);
        const float g = rbf(1.f / (1.f + __expf(-pre)));
        __builtin_amdgcn_raw_buffer_store_b32(__float_as_uint(g), wsr, WS_GATE + (u32)(mi * 128 + 64 * wt + lane) * 4, 0, 16);
      }
      if (lane == 0) __hip_atomic_fetch_add((LAS u32*)(size_t)(lds0 + CT_R), 1u, RLX_WG);
      STAMP(3);
    }
    __builtin_amdgcn_s_setprio(CVPRIO);
    if (regular) {
      WAIT_VM(0);
      if (lds_arrive(lds0 + CT_H, lane) == 3 && lane == 0) put_flag(ws + WS_FLAGH + (mi * 16 + ni) * 8, tag);
      STAMP(4);
    }
    {
      u32x4 uu[3], dd[3];
#pragma unroll
      for (int i = 0; i < 3; ++i) {
        const int c = 4 * i + q4 < 10 ? 4 * i + q4 : 9;
        uu[i] = *(const u32x4*)(P.u + (size_t)(64 * m64 + 16 * qw + l15) * D + 80 * n80 + 8 * c);
        dd[i] = *(const u32x4*)(P.dsk + 80 * n80 + 8 * c);
      }
      const u32 b2v = P.b2[80 * n80 + (tid < 80 ? tid : 0)];
      if (regular) {
        lds_wait(lds0 + CT_R, 4u);
      } else {
        lds_wait(lds0 + CT_PAIR, 2); lds_wait(lds0 + CT_PAIR + 4, 2);
      }
#pragma unroll
      for (int i = 0; i < 3; ++i) {
        const int c = 4 * i + q4 < 10 ? 4 * i + q4 : 9;
        u32x4 v;
#pragma unroll
        for (int e = 0; e < 4; ++e) v[e] = pk_bf16(bflo(uu[i][e]) * bflo(dd[i][e]), bfhi(uu[i][e]) * bfhi(dd[i][e]));
        ST(u32x4, lds0 + LDS_GA + (16 * qw + l15) * 160 + c * 16) = v;
      }
      if (tid < 80) ST(float, lds0 + LDS_B2 + tid * 4) = bf2f(b2v);
    }
    lds_wait(lds0 + CT_Q, 4u);
    STAMP(8);
    {
      f32x4 acc[8], acx[8];
#pragma unroll
      for (int j = 0; j < 8; ++j) { acc[j] = (f32x4){0.f, 0.f, 0.f, 0.f}; acx[j] = acc[j]; }
      const int z = l15 + 8 * q4 + 1;
      const u32 oa = CH_U + (z & 3) * UCP + (z & ~3) * 2, ob = CH_K + (2032 - 16 * l15 + 8 * q4) * 2 - 512 * 7;
      const u32 b0 = chb + half * 256, b1 = lds0 + REG_B + qw * 128;
      u32 pa = b0 + oa, pa8 = pa + 8, pb = b0 + ob, xa = b1 + oa, xa8 = xa + 8, xb = b1 + ob;
      asm volatile("" : "+v"(pa8), "+v"(xa8));
      ConvFrags f;
#pragma unroll
      for (int k = 0; k < 8; ++k) { f.A[0][k] = mk8(LD(u32x2, pa + 512 * k), LD(u32x2, pa8 + 512 * k)); f.B[0][7 - k] = LD(bf16x8, pb + 512 * k); }
      SB();
      conv_step<true>(f, 0, pa + 64, pa8 + 64, pb + 64, acc);
      conv_step<true>(f, 1, pa + 128, pa8 + 128, pb + 128, acc);
      conv_step<true>(f, 0, pa + 192, pa8 + 192, pb + 192, acc);
      conv_step<true>(f, 1, xa, xa8, xb, acc);
      if (hasx) {
        conv_step<true>(f, 0, xa + 64, xa8 + 64, xb + 64, acx);
        conv_step<false>(f, 1, 0, 0, 0, acx);
      }
      STAMP(9);
      if (half) {
#pragma unroll
        for (int j = 0; j < 4; ++j) { const f32x4 t = acc[j]; acc[j] = acc[4 + j]; acc[4 + j] = t; asm volatile("" : "+v"(acc[j]), "+v"(acc[4 + j])); }
      }
      const u32 x1 = lds0 + LDS_X1 + lane * 16, x2 = lds0 + LDS_X2 + lane * 16;
#pragma unroll
      for (int j = 0; j < 4; ++j) ST(f32x4, x1 + qw * 4096 + j * 1024) = acc[4 + j];
      if (hasx) {
#pragma unroll
        for (int j = 0; j < 8; ++j) ST(f32x4, x2 + qw * 8192 + j * 1024) = acx[j];
      }
      lds_arrive(lds0 + CT_Q, lane);
      lds_wait(lds0 + CT_Q, 8u);
      const u32 yo = (u32)(16 * l15 + 4 * q4) * 2;
#pragma unroll
      for (int j = 0; j < 4; ++j) {
        const f32x4 v = acc[j] + LD(f32x4, x1 + (qw ^ 1) * 4096 + j * 1024);
        __builtin_amdgcn_raw_buffer_store_b64((u32x2){pk_bf16(v[0], v[1]), pk_bf16(v[2], v[3])}, wsr, yo,
                                              WS_YT + (u32)(ch * (L * 2) + (4 * half + j) * 512), 16);
      }
      if (hasx) {
#pragma unroll
        for (int j = 0; j < 2; ++j) {
          const u32 o = x2 + (2 * qw + j) * 1024;
          const f32x4 v = (LD(f32x4, o) + LD(f32x4, o + 8192)) + (LD(f32x4, o + 16384) + LD(f32x4, o + 24576));
          __builtin_amdgcn_raw_buffer_store_b64((u32x2){pk_bf16(v[0], v[1]), pk_bf16(v[2], v[3])}, wsr, yo,
                                                WS_YT + (u32)((80 * xcd + 64 + (r >> 1)) * (L * 2) + (2 * qw + j) * 512), 16);
        }
      }
      WAIT_VM(0);
      if (lds_arrive(lds0 + CT_Y, lane) == 3u && lane == 0) put_flag(ws + WS_FLAGY + (xcd * 32 + r) * 8, tag);
      STAMP(10);
    }
    if (qw == 0) {
      lds_wait(lds0 + CT_Y, 4u);
      poll_flags<8>(lane < 32 ? ws + WS_FLAGY + (n80 * 32 + lane) * 8 : ws + WS_FLAGH + ((m64 >> 1) * 16 + 13) * 8, tag);
      if (lane == 0) __hip_atomic_store((LAS u32*)(size_t)(lds0 + CT_FY), 1u, RLX_WG);
    } else {
      lds_wait(lds0 + CT_FY, 1u);
    }
    STAMP(11);
    const float g = __uint_as_float(__builtin_amdgcn_raw_buffer_load_b32(wsr, (16 * qw + l15) * 4, WS_GATE + (u32)(64 * m64) * 4, 16));
    u32x4 yy[3];
#pragma unroll
    for (int i = 0; i < 3; ++i) {
      const int id = tid + 256 * i < 640 ? tid + 256 * i : 639;
      yy[i] = __builtin_amdgcn_raw_buffer_load_b128(wsr, (id >> 3) * (L * 2) + (id & 7) * 16, WS_YT + (u32)(80 * n80 * (L * 2) + 64 * m64 * 2), 16);
    }
    if (q4 == 0) ST(float, lds0 + LDS_OMG + (16 * qw + l15) * 4) = rbf(1.f - g);
#pragma unroll
    for (int i = 0; i < 3; ++i) {
      const int id = tid + 256 * i < 640 ? tid + 256 * i : 639;
      ST(u32x4, lds0 + LDS_YT + (id >> 3) * 144 + (id & 7) * 16) = yy[i];
    }
    lds_arrive(lds0 + CT_A, lane);
    lds_wait(lds0 + CT_A, 4u);
#pragma unroll
    for (int i = 0; i < 3; ++i) {
      const int c = 4 * i + q4 < 10 ? 4 * i + q4 : 9;
      const u32 p = lds0 + LDS_YT + (8 * c + (l15 >> 2)) * 144 + qw * 32 + 8 * (lane & 3);
      const u32x2 ya = tr_read(p), yb = tr_read(p + 4 * 144);
      const u32 ga = lds0 + LDS_GA + (16 * qw + l15) * 160 + c * 16;
      u32x4 z = LD(u32x4, ga);
#pragma unroll
      for (int e = 0; e < 4; ++e) {
        const u32 y = e < 2 ? ya[e] : yb[e - 2];
        z[e] = pk_bf16(g * (bflo(y) + bflo(z[e])), g * (bfhi(y) + bfhi(z[e])));
      }
      if (4 * i + q4 < 10) ST(u32x4, ga) = z;
    }
    if (bid == 0 && wave == 3) {
#pragma unroll 1
      for (int i = 0; i < 4; ++i) poll_flags<16>(ws + WS_FLAGY + (i * 64 + lane) * 8, tag);
      if (lane == 0) put_flag(ws, tag);
    }
  }
  STAMP(13);
  bar();
  STAMP(14);
#pragma unroll 1
  for (int id = tid; id < 640; id += 512) {
    const int t = (id * 6554) >> 16, c8 = id - 10 * t;
    f32x4 s0 = LD(f32x4, lds0 + LDS_B2 + c8 * 32), s1 = LD(f32x4, lds0 + LDS_B2 + c8 * 32 + 16);
#pragma unroll
    for (int v = 0; v < 4; ++v) {
      const u32 p = lds0 + REG_B + v * SLAB + t * 336 + c8 * 32;
      s0 += LD(f32x4, p); s1 += LD(f32x4, p + 16);
    }
    const u32x4 ga = LD(u32x4, lds0 + LDS_GA + t * 160 + c8 * 16);
    const float om = LD(float, lds0 + LDS_OMG + t * 4);
    u32x4 o;
#pragma unroll
    for (int e = 0; e < 4; ++e) {
      const float x0 = e < 2 ? s0[2 * e] : s1[2 * e - 4], x1 = e < 2 ? s0[2 * e + 1] : s1[2 * e - 3];
      o[e] = pk_bf16(bflo(ga[e]) + om * x0, bfhi(ga[e]) + om * x1);
    }
    *(u32x4*)(P.out + (size_t)(64 * m64 + t) * D + 80 * n80 + 8 * c8) = o;
  }
  PADTO(4000);
}

extern "C" void kernel_launch(void* const* d_in, const int* in_sizes, int n_in, void* d_out, int out_size,
                              void* d_ws, size_t ws_size, hipStream_t stream) {
  Params P;
  P.u = (const u16*)d_in[0]; P.lar = (const u16*)d_in[1]; P.aim = (const u16*)d_in[2]; P.cre = (const u16*)d_in[3];
  P.cim = (const u16*)d_in[4]; P.ldt = (const u16*)d_in[5]; P.dsk = (const u16*)d_in[6]; P.w1 = (const u16*)d_in[8];
  P.b1 = (const u16*)d_in[9]; P.w2 = (const u16*)d_in[10]; P.b2 = (const u16*)d_in[11]; P.gw = (const u16*)d_in[12];
  P.out = (u16*)d_out; P.ws = (char*)d_ws;
  (void)hipFuncSetAttribute((const void*)fused, hipFuncAttributeMaxDynamicSharedMemorySize, LDS_TOTAL);
  fused<<<256, 512, LDS_TOTAL, stream>>>(P);
}
```

```cpp
#include <hip/hip_runtime.h>
#include <hip/hip_bf16.h>

typedef unsigned short u16;
typedef unsigned int u32;
typedef unsigned long long u64;
typedef short s16x4 __attribute__((ext_vector_type(4)));
typedef __bf16 bf16x2 __attribute__((ext_vector_type(2)));
typedef __bf16 bf16x8 __attribute__((ext_vector_type(8)));
typedef _Float16 f16x2 __attribute__((ext_vector_type(2)));
typedef _Float16 f16x8 __attribute__((ext_vector_type(8)));
typedef float f32x2 __attribute__((ext_vector_type(2)));
typedef float f32x4 __attribute__((ext_vector_type(4)));
typedef u32 u32x2 __attribute__((ext_vector_type(2)));
typedef int i32x4 __attribute__((ext_vector_type(4)));
typedef u32 u32x4 __attribute__((ext_vector_type(4)));
typedef __amdgpu_buffer_rsrc_t rsrc_t;
#define LAS __attribute__((address_space(3)))
#define GAS __attribute__((address_space(1)))
#define RLX_AGENT __ATOMIC_RELAXED, __HIP_MEMORY_SCOPE_AGENT
#define RLX_WG __ATOMIC_RELAXED, __HIP_MEMORY_SCOPE_WORKGROUP
#define DEV __device__ __forceinline__
#define HSLEEP 40
#define G2PRIO 1
#define CVPRIO 2
#define KTPRIO 0
#define G2DP 4
#define YGATE 8
#define YPS 4
#define HWAVE 0
#define INH 1
#define INY 0
#define HPS 4
#define SCONS 1
#define STLATE 0
#define STAG 0
#define SB() __builtin_amdgcn_sched_barrier(0)

constexpr int L = 2048, D = 640, NM = 128, F = 1728;
constexpr u32 WS_FLAGK = 2048;
constexpr u32 WS_FLAGS = 4096;
constexpr u32 WS_FLAGH = WS_FLAGS + 2048;
constexpr u32 WS_FLAGY = WS_FLAGH + 2048;
constexpr u32 WS_INH = 16384;
constexpr u32 WS_INY = 49152;
constexpr u32 WS_GATE = 114688;
constexpr u32 WS_KX = 131072;
constexpr u32 WS_UT = WS_KX + 128 * 4096;
constexpr u32 WS_YT = WS_UT + D * L * 2;
constexpr u32 WS_HT = WS_YT + D * L * 2;
constexpr u32 WS_W2T = WS_HT + 32 * 54 * 4096;
constexpr u32 WS_END = WS_W2T + 8 * 54 * 5120;
#define STAMP(k)
constexpr u32 SCR_W = 15616;
constexpr u32 REG_A = 0, REG_B = 65536;
constexpr u32 SLAB = 64 * 336;
constexpr u32 LDS_SIDE = 131072;
constexpr u32 LDS_X1 = REG_B + 24576, LDS_X2 = REG_B + 40960;
constexpr u32 LDS_GW = LDS_SIDE + 20480;
constexpr u32 LDS_B1 = LDS_GW + 2048;
constexpr u32 LDS_PAR = LDS_B1 + 1024;
constexpr u32 LDS_GA = LDS_GW;
constexpr u32 LDS_OMG = LDS_GA + 10240;
constexpr u32 LDS_B2 = LDS_OMG + 256;
constexpr u32 LDS_CTL = LDS_B2 + 320;
constexpr u32 LDS_TOTAL = LDS_CTL + 64;
#define DBG(i)
constexpr u32 CH_KC0 = 0, CH_K = 2576, CH_U = CH_K + 4608, UCP = 4160, CH_KC1 = 2 * SCR_W - 2576;
constexpr u32 LDS_YT = 0;
constexpr u32 CT_PAIR = LDS_CTL, CT_H = LDS_CTL + 16, CT_Y = LDS_CTL + 20, CT_R = LDS_CTL + 24, CT_FH = LDS_CTL + 28, CT_FY = LDS_CTL + 32, CT_FS = LDS_CTL + 36, CT_Q = LDS_CTL + 40, CT_A = LDS_CTL + 48, CT_S = LDS_CTL + 52, CT_FW = LDS_CTL + 56;

struct Params {
  const u16 *u, *lar, *aim, *cre, *cim, *ldt, *dsk, *w1, *b1, *w2, *b2, *gw;
  u16* out;
  char* ws;
};

DEV float bf2f(u32 x) { return __uint_as_float(x << 16); }
DEV float bflo(u32 x) { return __uint_as_float(x << 16); }
DEV float bfhi(u32 x) { return __uint_as_float(x & 0xffff0000u); }
DEV u32 pk_bf16(float a, float b) { f32x2 v = {a, b}; bf16x2 r = __builtin_convertvector(v, bf16x2); return __builtin_bit_cast(u32, r); }
DEV float rbf(float a) { return bflo(pk_bf16(a, a)); }
DEV u32 pk_f16(f32x2 v) { f16x2 r = __builtin_convertvector(v, f16x2); return __builtin_bit_cast(u32, r); }
DEV f32x2 cmul(f32x2 a, f32x2 b) {
  f32x2 t, r;
  asm("v_pk_mul_f32 %0, %1, %2 op_sel:[0,0] op_sel_hi:[0,1]" : "=v"(t) : "v"(a), "v"(b));
  asm("v_pk_fma_f32 %0, %1, %2, %3 op_sel:[1,1,0] op_sel_hi:[1,0,1] neg_lo:[1,0,0]" : "=v"(r) : "v"(a), "v"(b), "v"(t));
  return r;
}
DEV f32x2 cexp_p(float ar, float ai, float p) {
  float e = __builtin_amdgcn_exp2f(ar * p * 1.44269504088896341f);
  float t = ai * p;
  float hi = t * 0.15915494309189535f;
  float lo = __builtin_fmaf(t, 0.15915494309189535f, -hi);
  lo = __builtin_fmaf(t, 6.4206383e-9f, lo);
  float fr = (hi - __builtin_rintf(hi)) + lo;
  return (f32x2){e * __builtin_amdgcn_cosf(fr), e * __builtin_amdgcn_sinf(fr)};
}
#define WAIT_VM(n) asm volatile("s_waitcnt vmcnt(" #n ")" ::: "memory")
#define WAIT_LGKM0() asm volatile("s_waitcnt lgkmcnt(0)" ::: "memory")
DEV void bar() { asm volatile("s_waitcnt lgkmcnt(0)" ::: "memory"); __builtin_amdgcn_s_barrier(); asm volatile("" ::: "memory"); }
DEV void rawbar() { asm volatile("" ::: "memory"); __builtin_amdgcn_s_barrier(); asm volatile("" ::: "memory"); }

DEV void glds1(const void* base, u32 voff, u32 lds) {
  u32 keep;
  asm volatile("s_mov_b32 %0, m0\n\ts_mov_b32 m0, %3\n\ts_nop 0\n\tglobal_load_lds_dwordx4 %1, %2\n\ts_mov_b32 m0, %0"
               : "=&s"(keep) : "v"(voff), "s"(base), "s"(lds) : "memory");
}
DEV void glds1p(const void* p, u32 lds) {
  u32 keep;
  asm volatile("s_mov_b32 %0, m0\n\ts_mov_b32 m0, %2\n\ts_nop 0\n\tglobal_load_lds_dwordx4 %1, off\n\ts_mov_b32 m0, %0"
               : "=&s"(keep) : "v"(p), "s"(lds) : "memory");
}
DEV void glds4(rsrc_t rs, u32 v0, u32 v1, u32 v2, u32 v3, u32 s, u32 step, u32 lds) {
  u32 keep;
  asm volatile("s_mov_b32 %0, m0\n\ts_mov_b32 m0, %10\n\ts_nop 0\n\t"
               "buffer_load_dwordx4 %1, %5, %6 offen lds\n\t"
               "buffer_load_dwordx4 %2, %5, %7 offen offset:1024 lds\n\t"
               "buffer_load_dwordx4 %3, %5, %8 offen offset:2048 lds\n\t"
               "buffer_load_dwordx4 %4, %5, %9 offen offset:3072 lds\n\t"
               "s_mov_b32 m0, %0"
               : "=&s"(keep) : "v"(v0), "v"(v1), "v"(v2), "v"(v3), "s"(rs), "s"(s), "s"(s + step),
                 "s"(s + 2 * step), "s"(s + 3 * step), "s"(lds) : "memory");
}
DEV u32x2 tr_read(u32 a) {
  s16x4 r = __builtin_amdgcn_ds_read_tr16_b64_v4i16((LAS s16x4*)(size_t)a);
  return __builtin_bit_cast(u32x2, r);
}
DEV bf16x8 mk8(u32x2 a, u32x2 b) { u32x4 v = {a.x, a.y, b.x, b.y}; return __builtin_bit_cast(bf16x8, v); }
DEV f32x4 mma(bf16x8 a, bf16x8 b, f32x4 c) { return __builtin_amdgcn_mfma_f32_16x16x32_bf16(a, b, c, 0, 0, 0); }
#define LD(T, a) (*(const LAS T*)(size_t)(a))
#define ST(T, a) (*(LAS T*)(size_t)(a))

template <int SLEEP>
DEV void poll_flags(const char* p, u64 tag) {
#pragma unroll 1
  for (u32 spins = 0; spins < (1u << 18); ++spins) {
    u64 v = __hip_atomic_load((const GAS u64*)p, RLX_AGENT);
    if (__all(v == tag)) break;
    __builtin_amdgcn_s_sleep(SLEEP);
  }
  __builtin_amdgcn_fence(__ATOMIC_ACQUIRE, "wavefront");
  asm volatile("" ::: "memory");
}
DEV void put_flag(char* p, u64 tag) { __hip_atomic_store((GAS u64*)p, tag, RLX_AGENT); }
DEV u32 lds_arrive(u32 a, int lane) {
  WAIT_LGKM0();
  u32 r = 0;
  if (lane == 0) r = __hip_atomic_fetch_add((LAS u32*)(size_t)a, 1u, RLX_WG);
  return __builtin_amdgcn_readfirstlane(r);
}
DEV void lds_wait(u32 a, u32 target) {
#pragma unroll 1
  for (u32 spins = 0; spins < (1u << 22); ++spins) {
    u32 v = __builtin_amdgcn_readfirstlane(__hip_atomic_load((LAS u32*)(size_t)a, RLX_WG));
    if (v >= target) break;
    __builtin_amdgcn_s_sleep(1);
  }
  asm volatile("" ::: "memory");
}

DEV void kgen(u32 pp, float ldt, u32 (&pk)[57], float& kscale) {
  const float dt = rbf(expf(ldt));
  const float ar = -rbf(expf(bf2f(LD(u16, pp)))) * dt, ai = bf2f(LD(u16, pp + 128)) * dt;
  const float cr = 2.f * bf2f(LD(u16, pp + 256)) * dt, ci = 2.f * bf2f(LD(u16, pp + 384)) * dt;
  u32 mx = max(__float_as_uint(cr) & 0x7fffffffu, __float_as_uint(ci) & 0x7fffffffu);
  mx = max(mx, (u32)__builtin_amdgcn_update_dpp(0, mx, 0x128, 0xf, 0xf, false));
  mx = max(mx, (u32)__builtin_amdgcn_update_dpp(0, mx, 0x124, 0xf, 0xf, false));
  mx = max(mx, (u32)__builtin_amdgcn_update_dpp(0, mx, 0x122, 0xf, 0xf, false));
  mx = max(mx, (u32)__builtin_amdgcn_update_dpp(0, mx, 0x121, 0xf, 0xf, false));
  u32 eb = max(max((u32)__builtin_amdgcn_readlane(mx, 0), (u32)__builtin_amdgcn_readlane(mx, 16)),
               max((u32)__builtin_amdgcn_readlane(mx, 32), (u32)__builtin_amdgcn_readlane(mx, 48))) >> 23;
  eb = eb < 40u ? 40u : (eb > 230u ? 230u : eb);
  const float inv = __uint_as_float((127u + 127u + 12u - eb) << 23);
  kscale = __uint_as_float((eb - 12u - 14u) << 23);
  const f32x2 e1 = cexp_p(ar, ai, 1.f), e8 = cexp_p(ar, -ai, 8.f), e128 = cexp_p(ar, ai, 128.f);
  {
    f32x2 G[16];
    const f32x2 e16 = cmul(e8, e8), e32 = cmul(e16, e16), e64 = cmul(e32, e32);
    G[0] = (f32x2){cr * inv, -ci * inv};
    G[1] = cmul(G[0], e8);
#pragma unroll
    for (int j = 0; j < 2; ++j) G[2 + j] = cmul(G[j], e16);
#pragma unroll
    for (int j = 0; j < 4; ++j) G[4 + j] = cmul(G[j], e32);
#pragma unroll
    for (int j = 0; j < 8; ++j) G[8 + j] = cmul(G[j], e64);
#pragma unroll
    for (int j = 0; j < 16; ++j) pk[j] = pk_f16(G[j]);
  }
  f32x2 Pw[8], Q[17];
  const f32x2 e2 = cmul(e1, e1), e4 = cmul(e2, e2);
  Pw[1] = e1; Pw[2] = e2; Pw[3] = cmul(e1, e2); Pw[4] = e4; Pw[5] = cmul(e1, e4); Pw[6] = cmul(e2, e4); Pw[7] = cmul(Pw[3], e4);
  const f32x2 e256 = cmul(e128, e128), e512 = cmul(e256, e256), e1024 = cmul(e512, e512);
  Q[1] = e128 * 16384.f; Q[2] = e256 * 16384.f; Q[4] = e512 * 16384.f; Q[8] = e1024 * 16384.f;
  Q[3] = cmul(Q[1], e256);
  Q[5] = cmul(Q[1], e512); Q[6] = cmul(Q[2], e512); Q[7] = cmul(Q[3], e512);
#pragma unroll
  for (int j = 1; j < 9; ++j) Q[8 + j] = cmul(Q[j], e1024);
  pk[16] = 0x00007400u;
#pragma unroll
  for (int j = 1; j < 8; ++j) pk[16 + j] = pk_f16(Pw[j] * 16384.f);
  pk[24] = pk_f16(Q[1]);
#pragma unroll
  for (int j = 1; j < 8; ++j) pk[24 + j] = pk_f16(cmul(Q[1], Pw[j]));
#pragma unroll
  for (int i = 2; i < 4; ++i) {
    pk[32 + 4 * (i - 2)] = pk_f16(Q[i]);
#pragma unroll
    for (int j = 1; j < 4; ++j) pk[32 + 4 * (i - 2) + j] = pk_f16(cmul(Q[i], Pw[2 * j]));
  }
#pragma unroll
  for (int i = 4; i < 8; ++i) { pk[40 + 2 * (i - 4)] = pk_f16(Q[i]); pk[41 + 2 * (i - 4)] = pk_f16(cmul(Q[i], Pw[4])); }
#pragma unroll
  for (int i = 8; i < 17; ++i) pk[48 + i - 8] = pk_f16(Q[i]);
}
template <int OFF>
DEV void put_rows(u32 base, const u32 (&pk)[57]) {
  u32 keep;
  asm volatile("s_mov_b32 %0, m0\n\ts_mov_b32 m0, %20\n\ts_nop 0\n\t"
               "ds_write_addtid_b32 %1 offset:%c21+0\n\t"
               "ds_write_addtid_b32 %2 offset:%c21+272\n\t"
               "ds_write_addtid_b32 %3 offset:%c21+544\n\t"
               "ds_write_addtid_b32 %4 offset:%c21+816\n\t"
               "ds_write_addtid_b32 %5 offset:%c21+1088\n\t"
               "ds_write_addtid_b32 %6 offset:%c21+1360\n\t"
               "ds_write_addtid_b32 %7 offset:%c21+1632\n\t"
               "ds_write_addtid_b32 %8 offset:%c21+1904\n\t"
               "ds_write_addtid_b32 %9 offset:%c21+2176\n\t"
               "ds_write_addtid_b32 %10 offset:%c21+2448\n\t"
               "ds_write_addtid_b32 %11 offset:%c21+2720\n\t"
               "ds_write_addtid_b32 %12 offset:%c21+2992\n\t"
               "ds_write_addtid_b32 %13 offset:%c21+3264\n\t"
               "ds_write_addtid_b32 %14 offset:%c21+3536\n\t"
               "ds_write_addtid_b32 %15 offset:%c21+3808\n\t"
               "ds_write_addtid_b32 %16 offset:%c21+4080\n\t"
               "ds_write_addtid_b32 %17 offset:%c21+4352\n\t"
               "ds_write_addtid_b32 %18 offset:%c21+4624\n\t"
               "ds_write_addtid_b32 %19 offset:%c21+4896\n\t"
               "s_mov_b32 m0, %0"
               : "=&s"(keep) : "v"(pk[0]), "v"(pk[1]), "v"(pk[2]), "v"(pk[3]), "v"(pk[4]), "v"(pk[5]), "v"(pk[6]), "v"(pk[7]), "v"(pk[8]), "v"(pk[9]), "v"(pk[10]), "v"(pk[11]), "v"(pk[12]), "v"(pk[13]), "v"(pk[14]), "v"(pk[15]), "v"(pk[16]), "v"(pk[17]), "v"(pk[18]), "s"(base), "i"(OFF) : "memory");
  asm volatile("s_mov_b32 %0, m0\n\ts_mov_b32 m0, %20\n\ts_nop 0\n\t"
               "ds_write_addtid_b32 %1 offset:%c21+5168\n\t"
               "ds_write_addtid_b32 %2 offset:%c21+5440\n\t"
               "ds_write_addtid_b32 %3 offset:%c21+5712\n\t"
               "ds_write_addtid_b32 %4 offset:%c21+5984\n\t"
               "ds_write_addtid_b32 %5 offset:%c21+6256\n\t"
               "ds_write_addtid_b32 %6 offset:%c21+6528\n\t"
               "ds_write_addtid_b32 %7 offset:%c21+6800\n\t"
               "ds_write_addtid_b32 %8 offset:%c21+7072\n\t"
               "ds_write_addtid_b32 %9 offset:%c21+7344\n\t"
               "ds_write_addtid_b32 %10 offset:%c21+7616\n\t"
               "ds_write_addtid_b32 %11 offset:%c21+7888\n\t"
               "ds_write_addtid_b32 %12 offset:%c21+8160\n\t"
               "ds_write_addtid_b32 %13 offset:%c21+8432\n\t"
               "ds_write_addtid_b32 %14 offset:%c21+8704\n\t"
               "ds_write_addtid_b32 %15 offset:%c21+8976\n\t"
               "ds_write_addtid_b32 %16 offset:%c21+9248\n\t"
               "ds_write_addtid_b32 %17 offset:%c21+9520\n\t"
               "ds_write_addtid_b32 %18 offset:%c21+9792\n\t"
               "ds_write_addtid_b32 %19 offset:%c21+10064\n\t"
               "s_mov_b32 m0, %0"
               : "=&s"(keep) : "v"(pk[19]), "v"(pk[20]), "v"(pk[21]), "v"(pk[22]), "v"(pk[23]), "v"(pk[24]), "v"(pk[25]), "v"(pk[26]), "v"(pk[27]), "v"(pk[28]), "v"(pk[29]), "v"(pk[30]), "v"(pk[31]), "v"(pk[32]), "v"(pk[33]), "v"(pk[34]), "v"(pk[35]), "v"(pk[36]), "v"(pk[37]), "s"(base), "i"(OFF) : "memory");
  asm volatile("s_mov_b32 %0, m0\n\ts_mov_b32 m0, %20\n\ts_nop 0\n\t"
               "ds_write_addtid_b32 %1 offset:%c21+10336\n\t"
               "ds_write_addtid_b32 %2 offset:%c21+10608\n\t"
               "ds_write_addtid_b32 %3 offset:%c21+10880\n\t"
               "ds_write_addtid_b32 %4 offset:%c21+11152\n\t"
               "ds_write_addtid_b32 %5 offset:%c21+11424\n\t"
               "ds_write_addtid_b32 %6 offset:%c21+11696\n\t"
               "ds_write_addtid_b32 %7 offset:%c21+11968\n\t"
               "ds_write_addtid_b32 %8 offset:%c21+12240\n\t"
               "ds_write_addtid_b32 %9 offset:%c21+12512\n\t"
               "ds_write_addtid_b32 %10 offset:%c21+12784\n\t"
               "ds_write_addtid_b32 %11 offset:%c21+13056\n\t"
               "ds_write_addtid_b32 %12 offset:%c21+13328\n\t"
               "ds_write_addtid_b32 %13 offset:%c21+13600\n\t"
               "ds_write_addtid_b32 %14 offset:%c21+13872\n\t"
               "ds_write_addtid_b32 %15 offset:%c21+14144\n\t"
               "ds_write_addtid_b32 %16 offset:%c21+14416\n\t"
               "ds_write_addtid_b32 %17 offset:%c21+14688\n\t"
               "ds_write_addtid_b32 %18 offset:%c21+14960\n\t"
               "ds_write_addtid_b32 %19 offset:%c21+15232\n\t"
               "s_mov_b32 m0, %0"
               : "=&s"(keep) : "v"(pk[38]), "v"(pk[39]), "v"(pk[40]), "v"(pk[41]), "v"(pk[42]), "v"(pk[43]), "v"(pk[44]), "v"(pk[45]), "v"(pk[46]), "v"(pk[47]), "v"(pk[48]), "v"(pk[49]), "v"(pk[50]), "v"(pk[51]), "v"(pk[52]), "v"(pk[53]), "v"(pk[54]), "v"(pk[55]), "v"(pk[56]), "s"(base), "i"(OFF) : "memory");
}
template <bool ANY>
DEV void ktable(u32 scr, u32 kb, const u32 (&pk)[57], float kscale, int lane) {
  const int l15 = lane & 15, q4 = lane >> 4;
  if (!ANY || scr < 65536) put_rows<0>(scr, pk); else put_rows<49152>(scr - 49152, pk);
  asm volatile("" ::: "memory");
  {
    f32x4 kc[3];
#pragma unroll
    for (int T = 0; T < 3; ++T) kc[T] = (f32x4){0.f, 0.f, 0.f, 0.f};
    const u32 rp = scr + l15 * 272 + q4 * 16;
    const u32 rp2 = scr + (48 + (l15 > 8 ? 8 : l15)) * 272 + q4 * 16;
    f16x8 fr[4][4];
#pragma unroll
    for (int ks = 0; ks < 4; ++ks) {
      fr[ks][0] = LD(f16x8, rp + ks * 64); fr[ks][1] = LD(f16x8, rp + 16 * 272 + ks * 64);
      fr[ks][2] = LD(f16x8, rp + 32 * 272 + ks * 64); fr[ks][3] = LD(f16x8, rp2 + ks * 64);
    }
    SB();
#pragma unroll
    for (int ks = 0; ks < 4; ++ks)
#pragma unroll
      for (int T = 0; T < 3; ++T) kc[T] = __builtin_amdgcn_mfma_f32_16x16x32_f16(fr[ks][1 + T], fr[ks][0], kc[T], 0, 0, 0);
    WAIT_LGKM0();
    ST(f32x4, kb + (128 * (q4 >> 1) + 8 * l15 + 4 * (q4 & 1)) * 4) = kc[0] * kscale;
    const f32x4 v1 = kc[1] * kscale, v2 = kc[2] * kscale;
    if (q4 < 2) {
      ST(f32x4, kb + (256 + 64 * q4 + 4 * l15) * 4) = v1;
#pragma unroll
      for (int rr = 0; rr < 4; ++rr) ST(float, kb + (512 + 16 * (4 * q4 + rr) + l15) * 4) = v2[rr];
    } else {
      ST(f32x2, kb + (384 + 64 * (q4 - 2) + 2 * l15) * 4) = (f32x2){v1[0], v1[1]};
      ST(f32x2, kb + (416 + 64 * (q4 - 2) + 2 * l15) * 4) = (f32x2){v1[2], v1[3]};
      if (lane == 32) ST(float, kb + 640 * 4) = v2[0];
    }
  }
}
DEV void expand(u32 chb, int half, int lane, u32x4& c0, u32x4& c1) {
  {
    auto tab = [&](int i) { return LD(float, chb + CH_KC0 + i * 4) + LD(float, chb + CH_KC1 + i * 4); };
    const int m = 255 - 64 * half - lane;
    const float a = tab(512 + m - 127), b = tab(512 + m - 128), c = (m & 1) ? a : b;
    const u32 bb = pk_bf16(b, b);
    c0 = (u32x4){pk_bf16(a, a), pk_bf16(a, c), bb, bb};
    if (half == 0) {
      const int i = 384 + 2 * (63 - lane);
      const float x = tab(i + 2), y = tab(i + 1), z = tab(i);
      c1 = (u32x4){pk_bf16(x, x), pk_bf16(y, y), pk_bf16(y, z), pk_bf16(z, z)};
    } else {
      const bool lo = lane >= 32;
      const int i = lo ? 8 * (63 - lane) : 256 + 4 * (31 - lane);
      const f32x4 s0 = LD(f32x4, chb + CH_KC0 + i * 4) + LD(f32x4, chb + CH_KC1 + i * 4);
      const f32x4 s1 = LD(f32x4, chb + CH_KC0 + i * 4 + 16) + LD(f32x4, chb + CH_KC1 + i * 4 + 16);
      c1 = (u32x4){pk_bf16(lo ? s1[3] : s1[0], lo ? s1[2] : s0[3]), pk_bf16(lo ? s1[1] : s0[2], lo ? s1[0] : s0[2]),
                   pk_bf16(lo ? s0[3] : s0[2], lo ? s0[2] : s0[1]), pk_bf16(lo ? s0[1] : s0[0], s0[0])};
    }
  }
}

typedef u32x2 __attribute__((aligned(4))) u32x2a;
DEV u32x2 ld2(u32 a) { return *(const LAS u32x2a*)(size_t)a; }
struct ConvFrags { bf16x8 A[2][8], B[2][8]; };
template <bool NEXT>
DEV void conv_step(ConvFrags& f, int set, u32 pa, u32 pa8, u32 pb, f32x4 (&acc)[8]) {
  u32x2 lo[8], hi[8];
#pragma unroll
  for (int g = 0; g < 12; ++g) {
    if (NEXT) {
#pragma unroll
      for (int d = 2 * g; d < 2 * g + 2; ++d) {
        const int k = d / 3;
        if (d % 3 == 0) lo[k] = ld2(pa + 512 * k);
        if (d % 3 == 1) { hi[k] = ld2(pa8 + 512 * k); f.A[set ^ 1][k] = mk8(lo[k], hi[k]); }
        if (d % 3 == 2) f.B[set ^ 1][7 - k] = LD(bf16x8, pb + 512 * k);
      }
    }
#pragma unroll
    for (int j = 3 * g; j < 3 * g + 3; ++j) {
      const int k = j < 1 ? 0 : j < 3 ? 1 : j < 6 ? 2 : j < 10 ? 3 : j < 15 ? 4 : j < 21 ? 5 : j < 28 ? 6 : 7, e = j - k * (k + 1) / 2;
      acc[e + 7 - k] = mma(f.A[set][e], f.B[set][7 - k], acc[e + 7 - k]);
    }
    if (NEXT) {
#pragma unroll
      for (int j = 0; j < 2; ++j) { __builtin_amdgcn_sched_group_barrier(0x8, 1, 0); __builtin_amdgcn_sched_group_barrier(0x100, 1, 0); }
      __builtin_amdgcn_sched_group_barrier(0x8, 1, 0);
    }
    SB();
  }
}

__global__ __launch_bounds__(512, 2) void fused(Params P) {
  extern __shared__ __attribute__((aligned(16))) char smem[];
  const int tid = threadIdx.x, lane = tid & 63;
  const int wave = __builtin_amdgcn_readfirstlane(tid >> 6);
  const int bid = blockIdx.x, xcd = bid & 7, r = bid >> 3;
  const bool regular = r < 28;
  const int quad = (wave >> 2) ^ 1, qw = wave & 3, half = qw & 1;
  const int l15 = lane & 15, q4 = lane >> 4;
  const u32 lds0 = (u32)(size_t)(LAS char*)smem;
  char* ws = P.ws;
  const int rq = (r * 37) >> 8, nl = r - 7 * rq, mi = (xcd >> 1) * 4 + rq, ni = (xcd & 1) * 7 + nl;
  const int rot = nl + 7 * rq - 10 * ((nl + 7 * rq) / 10);
  const int m64 = (xcd >> 1) * 8 + (r >> 2), n80 = (xcd & 1) * 4 + (r & 3);
  const rsrc_t wsr = __builtin_amdgcn_make_buffer_rsrc(ws, 0, WS_END, 0x00020000);
  const int chan0 = 80 * xcd + 2 * r;
  const u32 chb = lds0 + REG_A + (qw >> 1) * (2 * SCR_W);
  const int ch = chan0 + (qw >> 1);
  const bool hasx = !(r & 1);
  u64 tag;
  const char* hfl = INH ? ws + WS_INH + (m64 * 8 + n80) * 128 : ws + WS_FLAGH + (m64 >> 1) * 128;
  const char* yfl = INY ? ws + WS_INY + (m64 * 8 + n80) * 256 : ws + WS_FLAGY + n80 * 256;

  auto par_load = [&](int ch, u32 dst) {
    const int a = (lane >> 3) & 3;
    const u16 *p0 = P.lar, *p1 = P.aim, *p2 = P.cre, *p3 = P.cim;
    asm volatile("" : "+s"(p0), "+s"(p1), "+s"(p2), "+s"(p3));
    const u16* p = a == 0 ? p0 : a == 1 ? p1 : a == 2 ? p2 : p3;
    if (lane < 32) glds1p(p + ch * NM + 64 * half + (lane & 7) * 8, dst);
  };
  const bool isw = qw >= 2, side = !isw || m64 < 27;
  auto side_load = [&]() {
#pragma unroll
    for (int i = 0; i < 5; ++i) {
      const int o = i * 64 + lane, row = (o * 6554) >> 16, c = o - 10 * row;
      glds1(isw ? P.w2 : P.u, (u32)((m64 * 64 + half * 32 + row) * (D * 2) + n80 * 160 + c * 16), lds0 + LDS_SIDE + qw * 5120 + i * 1024);
    }
  };
  u32x4 sv[5];
  auto side_read = [&]() {
    const u32 p = lds0 + LDS_SIDE + qw * 5120 + ((isw ? 4 : 8) * q4 + (l15 >> 2)) * 160 + 8 * (lane & 3), p2 = p + (isw ? 16 * 160 : 4 * 160);
#pragma unroll
    for (int i = 0; i < 5; ++i) { const u32x2 a = tr_read(p + 32 * i), b = tr_read(p2 + 32 * i); sv[i] = (u32x4){a.x, a.y, b.x, b.y}; }
  };
  auto side_store = [&]() {
    const u32 o = isw ? WS_W2T + (u32)((n80 * 54 + 2 * m64 + half) * 5120) + lane * 16
                      : WS_UT + (u32)((80 * n80 + l15) * (L * 2) + (64 * m64 + 32 * half) * 2 + q4 * 16);
#pragma unroll
    for (int i = 0; i < 5; ++i) __builtin_amdgcn_raw_buffer_store_b128(sv[i], wsr, o + i * (isw ? 1024 : 16 * L * 2), 0, 16);
  };

  u32 ldt2 = 0;
  if (!regular) {
    const int xi = 4 * (r - 28) + 2 * quad + (qw >> 1);
    par_load(80 * xcd + 64 + xi, lds0 + LDS_PAR + (4 + wave) * 512);
    if (quad == 0) par_load(chan0 + (qw >> 1), lds0 + LDS_PAR + qw * 512);
    if (quad == 1 && side) side_load();
    if (tid < 16) ST(u32, lds0 + LDS_CTL + tid * 4) = 0;
    u32 ldx;
    asm volatile("s_load_dwordx2 %0, %3, 0x0\n\ts_load_dword %1, %4, 0x0\n\ts_load_dword %2, %5, 0x0\n\ts_waitcnt lgkmcnt(0)"
                 : "=&s"(tag), "=&s"(ldt2), "=&s"(ldx) : "s"(ws), "s"(P.ldt + chan0), "s"(P.ldt + 80 * xcd + 64 + (xi & ~1)) : "memory");
    tag += 1;
    if (quad == 1 && side) WAIT_VM(5); else WAIT_VM(0);
    u32 pk[57];
    float kscale;
    kgen(lds0 + LDS_PAR + (4 + wave) * 512 + lane * 2, (xi & 1) ? bfhi(ldx) : bflo(ldx), pk, kscale);
#pragma unroll
    for (int j = 0; j < 57; ++j) asm volatile("" : "+v"(pk[j]));
    WAIT_VM(0);
    STAMP(1);
    if (quad == 1 && side) { side_read(); side_store(); }
    const u32 xb = chb + (quad ? REG_B : 0);
    ktable<true>(xb + half * SCR_W, xb + (half ? CH_KC1 : CH_KC0), pk, kscale, lane);
    bar();
    u32x4 c0, c1;
    expand(xb, half, lane, c0, c1);
    const u32 ko = WS_KX + (u32)((xcd * 16 + xi) * 4096 + half * 1024) + lane * 16;
    __builtin_amdgcn_raw_buffer_store_b128(c0, wsr, ko, 0, 16);
    __builtin_amdgcn_raw_buffer_store_b128(c1, wsr, ko + 2048, 0, 16);
    WAIT_VM(0);
    bar();
    if (tid == 0) { put_flag(ws + WS_FLAGK + (xcd * 4 + r - 28) * 8, tag); put_flag(ws + WS_FLAGS + (n80 * 32 + m64) * 8, tag); }
    STAMP(2);
  }

  if (quad == 0) {
    const rsrc_t xr = __builtin_amdgcn_make_buffer_rsrc((void*)P.u, 0, L * D * 2, 0x00020000);
    const rsrc_t wr = __builtin_amdgcn_make_buffer_rsrc((void*)P.w1, 0, D * F * 2, 0x00020000);
    const u32 xv = (u32)((lane >> 3) * (D * 2) + ((lane & 7) ^ q4) * 16);
    const u32 wv = (u32)(q4 * (F * 2) + (l15 ^ (q4 << 2)) * 16);
    const u32 xs = (u32)((mi * 128 + 32 * qw) * (D * 2)), wso = (u32)(16 * qw * (F * 2) + ni * 256);
    auto stage = [&](int i) {
      const u32 dst = lds0 + ((i + 2) & 3) * 32768 + qw * 4096;
      const int k = i + rot >= 10 ? i + rot - 10 : i + rot;
      glds4(xr, xv, xv ^ 64, xv, xv ^ 64, xs + k * 128, 8 * D * 2 - 1024, dst);
      glds4(wr, wv, wv ^ 16, wv ^ 32, wv ^ 48, wso + k * (64 * F * 2), 4 * F * 2 - 1024, dst + 16384);
    };
    if (regular) {
      __builtin_amdgcn_s_setprio(3);
      rawbar();
      par_load(chan0 + (qw >> 1), lds0 + LDS_PAR + qw * 512);
      stage(0);
      if (qw == 0) glds1(P.b1, (u32)(ni * 256 + (lane & (ni == 13 ? 7 : 15)) * 16), lds0 + LDS_B1);
      if (qw == 1 && ni == 13) { glds1(P.gw, lane * 16, lds0 + LDS_GW); glds1(P.gw, 1024 + l15 * 16, lds0 + LDS_GW + 1024); }
      stage(1);
      asm volatile("s_load_dwordx2 %0, %2, 0x0\n\ts_load_dword %1, %3, 0x0\n\ts_waitcnt lgkmcnt(0)"
                   : "=&s"(tag), "=&s"(ldt2) : "s"(ws), "s"(P.ldt + chan0) : "memory");
      tag += 1;
      WAIT_VM(16);
    }
    u32 pk[57];
    float kscale;
    kgen(lds0 + LDS_PAR + qw * 512 + lane * 2, (qw >> 1) ? bfhi(ldt2) : bflo(ldt2), pk, kscale);
#pragma unroll
    for (int j = 0; j < 57; ++j) asm volatile("" : "+v"(pk[j]));
    STAMP(5);
    if (regular) {
      if (STLATE == 2) { WAIT_VM(8); if (lane == 0) __hip_atomic_fetch_add((LAS u32*)(size_t)(lds0 + CT_FS), 1u, RLX_WG); }
      stage(2);
#pragma unroll 1
      for (int s = 0; s < 7; ++s) {
        WAIT_VM(8);
        rawbar();
        stage(s + 3);
        if (!SCONS && s == 3 && tid == 256) put_flag(ws + WS_FLAGS + (n80 * 32 + m64) * 8, tag);
      }
      WAIT_VM(8);
      rawbar();
      WAIT_VM(0);
      rawbar();
    }
    STAMP(4);
    __builtin_amdgcn_s_setprio(KTPRIO);
    {
      const char* fp = lane < 32 ? ws + WS_FLAGS + (xcd * 32 + lane) * 8 : lane < 59 ? ws + WS_FLAGS + (n80 * 32 + lane - 32) * 8
                                                                                     : ws + WS_FLAGK + (xcd * 4 + (r >> 3)) * 8;
      const u64 f0 = __hip_atomic_load((const GAS u64*)fp, RLX_AGENT);
      ktable<false>(chb + half * SCR_W, chb + (half ? CH_KC1 : CH_KC0), pk, kscale, lane);
      if (!__all(f0 == tag)) {
        if (!regular) __builtin_amdgcn_s_sleep(HSLEEP);
        poll_flags<16>(fp, tag);
      }
      __builtin_amdgcn_fence(__ATOMIC_ACQUIRE, "wavefront");
      u32x4 ud[3], kx;
      u32 un[3];
      const u32 uo = WS_UT + (u32)(ch * (L * 2) + half * 2048);
#pragma unroll
      for (int i = 0; i < 2; ++i) {
        ud[i] = __builtin_amdgcn_raw_buffer_load_b128(wsr, lane * 16 + i * 1024, uo, 16);
        un[i] = __builtin_amdgcn_raw_buffer_load_b32(wsr, lane * 16 + i * 1024 + 16, uo, 16);
      }
      const u32 xo = WS_UT + (u32)((80 * xcd + 64 + (r >> 1)) * (L * 2) + qw * 1024);
      ud[2] = __builtin_amdgcn_raw_buffer_load_b128(wsr, lane * 16, xo, 16);
      un[2] = __builtin_amdgcn_raw_buffer_load_b32(wsr, lane * 16 + 16, xo, 16);
      kx = __builtin_amdgcn_raw_buffer_load_b128(wsr, lane * 16, WS_KX + (u32)((xcd * 16 + (r >> 1)) * 4096 + qw * 1024), 16);
      STAMP(1);
      lds_arrive(lds0 + CT_PAIR + (qw >> 1) * 4, lane);
      lds_wait(lds0 + CT_PAIR + (qw >> 1) * 4, 2);
      u32x4 c0, c1;
      expand(chb, half, lane, c0, c1);
      ST(u32x4, chb + CH_K + (64 * half + lane) * 16) = c0;
      ST(u32x4, chb + CH_K + (128 + 64 * half + lane) * 16) = c1;
      auto put2 = [&](u32 a, u32x4 d, u32 n) {
        ST(u32x4, a) = d;
        ST(u32x4, a + UCP) = (u32x4){__builtin_amdgcn_alignbit(d[1], d[0], 16), __builtin_amdgcn_alignbit(d[2], d[1], 16),
                                     __builtin_amdgcn_alignbit(d[3], d[2], 16), __builtin_amdgcn_alignbit(n, d[3], 16)};
      };
      auto edges = [&](u32 blk, u32x4 d) {
        const u32x4 z = {0, 0, 0, 0};
        if (lane < 2) put2(blk + CH_U + 16 - lane * 16, z, lane ? 0 : d[0]);
        else if (lane < 32) ST(u32x4, blk + CH_K + (254 + lane) * 16) = z;
      };
#pragma unroll
      for (int i = 0; i < 2; ++i) put2(chb + CH_U + 32 + half * 2048 + i * 1024 + lane * 16, ud[i], un[i]);
      if (half == 0) edges(chb, ud[0]);
      if (hasx) {
        if (regular) lds_wait(lds0 + CT_R, 4u);
        put2(lds0 + REG_B + CH_U + 32 + qw * 1024 + lane * 16, ud[2], un[2]);
        ST(u32x4, lds0 + REG_B + CH_K + qw * 1024 + lane * 16) = kx;
        if (qw == 0) edges(lds0 + REG_B, ud[2]);
      }
    }
    lds_arrive(lds0 + CT_Q, lane);
    STAMP(8);
    __builtin_amdgcn_s_setprio(G2PRIO);
    if (regular) lds_wait(lds0 + CT_R, 4u);
    constexpr int NS = 14, DP = G2DP;
    const int first = qw >> 1;
    const int k0 = (qw < 2 ? 14 * qw : 28 + 13 * (qw - 2)) - first;
    const u32 hb = WS_HT + (u32)((m64 * 54 + k0) * 4096), wb = WS_W2T + (u32)((n80 * 54 + k0) * 5120);
    const u32 lo = lane * 16;
    u32x4 fr[DP][9];
    auto load1 = [&](int s, int i) {
      const int j = s;
      fr[s % DP][i] = i < 5 ? __builtin_amdgcn_raw_buffer_load_b128(wsr, lo + (i & 3) * 1024, wb + j * 5120 + (i >> 2) * 4096, 16)
                            : __builtin_amdgcn_raw_buffer_load_b128(wsr, lo + (i - 5) * 1024, hb + j * 4096, 16);
    };
#pragma unroll
    for (int s = 0; s < DP - 1; ++s)
      if (s >= first) {
#pragma unroll
        for (int i = 0; i < 5; ++i) load1(s, i);
      }
    const u32 tch = __builtin_amdgcn_raw_buffer_load_b32(wsr, (u32)(((m64 & 7) * 270 + qw * 64 + lane) * 128), WS_W2T + (u32)(n80 * 54 * 5120), 16);
    STAMP(6);
    if (qw == 0) {
      if (regular) lds_wait(lds0 + CT_H, 4u);
      poll_flags<HPS>(hfl + (lane < 14 ? lane : 0) * 8, tag);
      if (lane == 0) __hip_atomic_store((LAS u32*)(size_t)(lds0 + CT_FH), 1u, RLX_WG);
    } else {
      lds_wait(lds0 + CT_FH, 1u);
    }
    asm volatile("" :: "v"(tch));
    STAMP(11);
#pragma unroll
    for (int s = 0; s < DP - 1; ++s)
      if (s >= first) {
#pragma unroll
        for (int i = 5; i < 9; ++i) load1(s, i);
      }
    f32x4 acc[5][4];
#pragma unroll
    for (int a = 0; a < 5; ++a)
#pragma unroll
      for (int b = 0; b < 4; ++b) acc[a][b] = (f32x4){0.f, 0.f, 0.f, 0.f};
    SB();
#pragma unroll
    for (int s = 0; s < NS; ++s) {
      if (s == YGATE && lane == 0) __hip_atomic_fetch_add((LAS u32*)(size_t)(lds0 + CT_FW), 1u, RLX_WG);
      if (s >= (STAG ? 3 : 1) || s >= first) {
#pragma unroll
        for (int a = 0; a < 5; ++a) {
          if (s + DP - 1 < NS) { load1(s + DP - 1, 2 * a); if (a < 4) load1(s + DP - 1, 2 * a + 1); }
#pragma unroll
          for (int b = 0; b < 4; ++b)
            acc[a][b] = mma(__builtin_bit_cast(bf16x8, fr[s % DP][a]), __builtin_bit_cast(bf16x8, fr[s % DP][5 + b]), acc[a][b]);
          SB();
        }
      } else {
        if (s + DP - 1 >= first) {
#pragma unroll
          for (int i = 0; i < 9; ++i) load1(s + DP - 1, i);
        }
        SB();
      }
    }
    STAMP(12);
    lds_wait(lds0 + CT_Y, 4u);
    const u32 sl = lds0 + REG_B + qw * SLAB + l15 * 336 + q4 * 16;
#pragma unroll
    for (int a = 0; a < 5; ++a)
#pragma unroll
      for (int b = 0; b < 4; ++b) ST(f32x4, sl + b * 16 * 336 + a * 64) = acc[a][b];
  } else {
    const bool lastn = ni == 13;
    u32x4 uu[3], dd[3];
    u32 b2v;
    auto ud_load = [&]() {
#pragma unroll
      for (int i = 0; i < 3; ++i) {
        const int c = 4 * i + q4 < 10 ? 4 * i + q4 : 9;
        uu[i] = *(const u32x4*)(P.u + (size_t)(64 * m64 + 16 * qw + l15) * D + 80 * n80 + 8 * c);
        dd[i] = *(const u32x4*)(P.dsk + 80 * n80 + 8 * c);
      }
      b2v = *(const u32*)(P.b2 + 80 * n80 + (tid < 80 ? tid & ~1 : 0));
    };
    auto ud_fence = [&]() { asm volatile("" : "+v"(uu[0]), "+v"(uu[1]), "+v"(uu[2]), "+v"(dd[0]), "+v"(dd[1]), "+v"(dd[2]), "+v"(b2v)); };
    if (!regular) { ud_load(); ud_fence(); }
    if (regular) {
      if (side) side_load();
      if (tid < 16) ST(u32, lds0 + LDS_CTL + tid * 4) = 0;
      bar();
      asm volatile("s_load_dwordx2 %0, %1, 0x0\n\ts_waitcnt lgkmcnt(0)" : "=&s"(tag) : "s"(ws) : "memory");
      tag += 1;
      WAIT_VM(0);
      STAMP(5);
      side_read();
      if (side) side_store();
      STAMP(6);
      __builtin_amdgcn_s_setprio(2);
      const int wf = qw & 1, wt = qw >> 1;
      u32 xa[2], wa[4][2];
      xa[0] = lds0 + 65536 + (u32)((64 * wt + l15) * 128 + ((q4 ^ (l15 >> 1)) * 16)); xa[1] = xa[0] ^ 64;
      f32x4 acc[4][4];
      if (!(lastn && wf)) {
#pragma unroll
        for (int t = 0; t < 2; ++t) {
          const int qq = l15 >> 2, p = lane & 3;
          const int row = 8 * q4 + 4 * t + qq, c = 8 * wf + (p >> 1);
          const u32 a0 = lds0 + 65536 + 16384u + 256u * row + 16u * (c ^ ((qq << 2) | ((2 * q4 + t) & 3))) + 8u * (p & 1);
#pragma unroll
          for (int ft = 0; ft < 4; ++ft) wa[ft][t] = a0 ^ (ft << 5);
        }
        bf16x8 A[2][4], B[2][4];
        auto rdf = [&](int set, int i, u32 so, int kk) {
          A[set][i] = mk8(tr_read(wa[i][0] + so + kk * 8192), tr_read(wa[i][1] + so + kk * 8192));
          B[set][i] = LD(bf16x8, xa[kk] + so + i * 2048);
        };
        auto hstep = [&](int set, u32 so, int kk) {
          constexpr int ord[16][2] = {{0, 0}, {0, 1}, {1, 0}, {1, 1}, {0, 2}, {1, 2}, {2, 0}, {2, 1},
                                      {2, 2}, {0, 3}, {1, 3}, {2, 3}, {3, 0}, {3, 1}, {3, 2}, {3, 3}};
#pragma unroll
          for (int g = 0; g < 4; ++g) {
            rdf(set ^ 1, g, so, kk);
#pragma unroll
            for (int j = 0; j < 4; ++j) {
              const int a = ord[4 * g + j][0], b = ord[4 * g + j][1];
              acc[a][b] = mma(A[set][a], B[set][b], acc[a][b]);
            }
#pragma unroll
            for (int j = 0; j < 3; ++j) { __builtin_amdgcn_sched_group_barrier(0x8, 1, 0); __builtin_amdgcn_sched_group_barrier(0x100, 1, 0); }
            __builtin_amdgcn_sched_group_barrier(0x8, 1, 0);
            SB();
          }
        };
        bar();
        STAMP(1);
#pragma unroll
        for (int i = 0; i < 4; ++i) rdf(0, i, 0, 0);
        if (STLATE == 1 && side) side_store();
#pragma unroll
        for (int ft = 0; ft < 4; ++ft) {
          const u32x2 bi = LD(u32x2, lds0 + LDS_B1 + (wf * 64 + ft * 16 + 4 * q4) * 2);
#pragma unroll
          for (int tt = 0; tt < 4; ++tt) acc[ft][tt] = (f32x4){bflo(bi.x), bfhi(bi.x), bflo(bi.y), bfhi(bi.y)};
        }
        auto flip = [&]() {
          xa[0] ^= 65536; xa[1] ^= 65536;
#pragma unroll
          for (int ft = 0; ft < 4; ++ft) { wa[ft][0] ^= 65536; wa[ft][1] ^= 65536; }
          SB();
        };
#pragma unroll 1
        for (int p = 0; p < 4; ++p) {
          hstep(0, 0, 1); hstep(1, 32768, 0);
          if (p == 1) WAIT_VM(0);
          rawbar();
          if (SCONS && p == 1 && tid == 0) put_flag(ws + WS_FLAGS + (n80 * 32 + m64) * 8, tag);
          SB();
          hstep(0, 32768, 1);
          flip();
          hstep(1, 0, 0);
          rawbar(); SB();
        }
        ud_load(); SB();
        hstep(0, 0, 1); hstep(1, 32768, 0); hstep(0, 32768, 1); hstep(1, 0, 0);
        ud_fence();
        STAMP(2);
        const u32 ho = WS_HT + (u32)(((2 * mi + wt) * 54 + 4 * ni + 2 * wf) * 4096);
#pragma unroll
        for (int kh = 0; kh < 2; ++kh)
#pragma unroll
          for (int tt = 0; tt < 4; ++tt) {
            u32x4 o;
#pragma unroll
            for (int h = 0; h < 2; ++h) {
              f32x4 v = __builtin_bit_cast(f32x4, __builtin_elementwise_max(__builtin_bit_cast(i32x4, acc[2 * kh + h][tt]), (i32x4){0, 0, 0, 0}));
              v = v * v;
              o[2 * h] = pk_bf16(v[0], v[1]); o[2 * h + 1] = pk_bf16(v[2], v[3]);
            }
            __builtin_amdgcn_raw_buffer_store_b128(o, wsr, ho + kh * 4096 + lane * 16 + tt * 1024, 0, 16);
          }
      } else {
#pragma unroll
        for (int tt = 0; tt < 4; ++tt) acc[0][tt] = (f32x4){0.f, 0.f, 0.f, 0.f};
#pragma unroll 1
        for (int s = 0; s < 10; ++s) {
          if (s == 3) WAIT_VM(0);
          if (s < 9) bar();
          if (STLATE == 1 && s == 0 && side) side_store();
          const int k = s + rot >= 10 ? s + rot - 10 : s + rot;
#pragma unroll
          for (int kk = 0; kk < 2; ++kk) {
            const bf16x8 Ag = LD(bf16x8, lds0 + LDS_GW + k * 128 + kk * 64 + q4 * 16);
#pragma unroll
            for (int tt = 0; tt < 4; ++tt) acc[0][tt] = mma(Ag, LD(bf16x8, (xa[kk] ^ (((s + 2) & 3) * 32768) ^ 65536) + tt * 2048), acc[0][tt]);
          }
        }
        ud_load(); ud_fence();
        float pre = q4 == 0 ? acc[0][0][0] : q4 == 1 ? acc[0][1][0] : q4 == 2 ? acc[0][2][0] : acc[0][3][0];
        pre = rbf(pre);
        const float g = rbf(1.f / (1.f + __expf(-pre)));
        __builtin_amdgcn_raw_buffer_store_b32(__float_as_uint(g), wsr, WS_GATE + (u32)(mi * 128 + 64 * wt + lane) * 4, 0, 16);
      }
      if (lane == 0) __hip_atomic_fetch_add((LAS u32*)(size_t)(lds0 + CT_R), 1u, RLX_WG);
      STAMP(3);
    }
    __builtin_amdgcn_s_setprio(CVPRIO);
    {
      if (regular) {
        lds_wait(lds0 + CT_R, 4u);
      } else {
        lds_wait(lds0 + CT_PAIR, 2); lds_wait(lds0 + CT_PAIR + 4, 2);
      }
#pragma unroll
      for (int i = 0; i < 3; ++i) {
        const int c = 4 * i + q4 < 10 ? 4 * i + q4 : 9;
        u32x4 v;
#pragma unroll
        for (int e = 0; e < 4; ++e) v[e] = pk_bf16(bflo(uu[i][e]) * bflo(dd[i][e]), bfhi(uu[i][e]) * bfhi(dd[i][e]));
        ST(u32x4, lds0 + LDS_GA + (16 * qw + l15) * 160 + c * 16) = v;
      }
      if (tid < 80) ST(float, lds0 + LDS_B2 + tid * 4) = (tid & 1) ? bfhi(b2v) : bflo(b2v);
    }
    if (regular) {
      WAIT_VM(0);
      if (lds_arrive(lds0 + CT_H, lane) == 3 && lane < 16) put_flag(ws + WS_INH + ((mi * 16 + lane) * 16 + ni) * 8, tag);
      STAMP(4);
    }
    lds_wait(lds0 + CT_Q, 4u);
    STAMP(8);
    {
      f32x4 acc[8], acx[8];
#pragma unroll
      for (int j = 0; j < 8; ++j) { acc[j] = (f32x4){0.f, 0.f, 0.f, 0.f}; acx[j] = acc[j]; }
      const int z = l15 + 8 * q4 + 1;
      const u32 oa = CH_U + (z & 1) * UCP + (z & ~1) * 2, ob = CH_K + (2032 - 16 * l15 + 8 * q4) * 2 - 512 * 7;
      const u32 b0 = chb + half * 256, b1 = lds0 + REG_B + qw * 128;
      u32 pa = b0 + oa, pa8 = pa + 8, pb = b0 + ob, xa = b1 + oa, xa8 = xa + 8, xb = b1 + ob;
      asm volatile("" : "+v"(pa8), "+v"(xa8));
      ConvFrags f;
#pragma unroll
      for (int k = 0; k < 8; ++k) { f.A[0][k] = mk8(ld2(pa + 512 * k), ld2(pa8 + 512 * k)); f.B[0][7 - k] = LD(bf16x8, pb + 512 * k); }
      SB();
      conv_step<true>(f, 0, pa + 64, pa8 + 64, pb + 64, acc);
      conv_step<true>(f, 1, pa + 128, pa8 + 128, pb + 128, acc);
      conv_step<true>(f, 0, pa + 192, pa8 + 192, pb + 192, acc);
      conv_step<true>(f, 1, xa, xa8, xb, acc);
      if (hasx) {
        conv_step<true>(f, 0, xa + 64, xa8 + 64, xb + 64, acx);
        conv_step<false>(f, 1, 0, 0, 0, acx);
      }
      STAMP(9);
      if (half) {
#pragma unroll
        for (int j = 0; j < 4; ++j) { const f32x4 t = acc[j]; acc[j] = acc[4 + j]; acc[4 + j] = t; asm volatile("" : "+v"(acc[j]), "+v"(acc[4 + j])); }
      }
      const u32 x1 = lds0 + LDS_X1 + lane * 16, x2 = lds0 + LDS_X2 + lane * 16;
#pragma unroll
      for (int j = 0; j < 4; ++j) ST(f32x4, x1 + qw * 4096 + j * 1024) = acc[4 + j];
      if (hasx) {
#pragma unroll
        for (int j = 0; j < 8; ++j) ST(f32x4, x2 + qw * 8192 + j * 1024) = acx[j];
      }
      lds_arrive(lds0 + CT_Q, lane);
      lds_wait(lds0 + CT_Q, 8u);
      auto y_store = [&](f32x4 v, f32x4 w, u32 o) {
        const auto a = __builtin_amdgcn_permlane16_swap(pk_bf16(v[0], v[1]), pk_bf16(w[0], w[1]), false, false);
        const auto b = __builtin_amdgcn_permlane16_swap(pk_bf16(v[2], v[3]), pk_bf16(w[2], w[3]), false, false);
        __builtin_amdgcn_raw_buffer_store_b128((u32x4){a[0], b[0], a[1], b[1]}, wsr, WS_YT + o + (q4 & 1) * 512 + (16 * l15 + 8 * (q4 >> 1)) * 2, 0, 16);
      };
#pragma unroll
      for (int j = 0; j < 4; j += 2)
        y_store(acc[j] + LD(f32x4, x1 + (qw ^ 1) * 4096 + j * 1024), acc[j + 1] + LD(f32x4, x1 + (qw ^ 1) * 4096 + j * 1024 + 1024),
                (u32)(ch * (L * 2) + (4 * half + j) * 512));
      if (hasx) {
        f32x4 v[2];
#pragma unroll
        for (int j = 0; j < 2; ++j) {
          const u32 o = x2 + (2 * qw + j) * 1024;
          v[j] = (LD(f32x4, o) + LD(f32x4, o + 8192)) + (LD(f32x4, o + 16384) + LD(f32x4, o + 24576));
        }
        y_store(v[0], v[1], (u32)((80 * xcd + 64 + (r >> 1)) * (L * 2) + 2 * qw * 512));
      }
      WAIT_VM(0);
      if (lds_arrive(lds0 + CT_Y, lane) == 3u) {
        if (lane == 32) put_flag(ws + WS_FLAGY + (xcd * 32 + r) * 8, tag);
        if (INY && lane < 32) put_flag(ws + WS_INY + ((lane * 8 + xcd) * 32 + r) * 8, tag);
      }
      STAMP(10);
    }
    if (qw == 0) {
      lds_wait(lds0 + CT_Y, 4u);
      lds_wait(lds0 + CT_FW, 4u);
      poll_flags<YPS>(lane < 32 ? yfl + lane * 8 : hfl + 13 * 8, tag);
      if (lane == 0) __hip_atomic_store((LAS u32*)(size_t)(lds0 + CT_FY), 1u, RLX_WG);
    } else {
      lds_wait(lds0 + CT_FY, 1u);
    }
    STAMP(11);
    const float g = __uint_as_float(__builtin_amdgcn_raw_buffer_load_b32(wsr, (16 * qw + l15) * 4, WS_GATE + (u32)(64 * m64) * 4, 16));
    u32x4 yy[3];
#pragma unroll
    for (int i = 0; i < 3; ++i) {
      const int id = tid + 256 * i < 640 ? tid + 256 * i : 639;
      yy[i] = __builtin_amdgcn_raw_buffer_load_b128(wsr, (id >> 3) * (L * 2) + (id & 7) * 16, WS_YT + (u32)(80 * n80 * (L * 2) + 64 * m64 * 2), 16);
    }
    if (q4 == 0) ST(float, lds0 + LDS_OMG + (16 * qw + l15) * 4) = rbf(1.f - g);
#pragma unroll
    for (int i = 0; i < 3; ++i) {
      const int id = tid + 256 * i < 640 ? tid + 256 * i : 639;
      ST(u32x4, lds0 + LDS_YT + (id >> 3) * 144 + (id & 7) * 16) = yy[i];
    }
    lds_arrive(lds0 + CT_A, lane);
    lds_wait(lds0 + CT_A, 4u);
#pragma unroll
    for (int i = 0; i < 3; ++i) {
      const int c = 4 * i + q4 < 10 ? 4 * i + q4 : 9;
      const u32 p = lds0 + LDS_YT + (8 * c + (l15 >> 2)) * 144 + qw * 32 + 8 * (lane & 3);
      const u32x2 ya = tr_read(p), yb = tr_read(p + 4 * 144);
      const u32 ga = lds0 + LDS_GA + (16 * qw + l15) * 160 + c * 16;
      u32x4 z = LD(u32x4, ga);
#pragma unroll
      for (int e = 0; e < 4; ++e) {
        const u32 y = e < 2 ? ya[e] : yb[e - 2];
        z[e] = pk_bf16(g * (bflo(y) + bflo(z[e])), g * (bfhi(y) + bfhi(z[e])));
      }
      if (4 * i + q4 < 10) ST(u32x4, ga) = z;
    }
    if (bid == 0 && wave == 3) {
#pragma unroll 1
      for (int i = 0; i < 4; ++i) poll_flags<16>(ws + WS_FLAGY + (i * 64 + lane) * 8, tag);
      if (lane == 0) put_flag(ws, tag);
    }
  }
  STAMP(13);
  bar();
  STAMP(14);
  auto fin = [&](int id) {
    const int t = (id * 6554) >> 16, c8 = id - 10 * t;
    f32x4 s0 = LD(f32x4, lds0 + LDS_B2 + c8 * 32), s1 = LD(f32x4, lds0 + LDS_B2 + c8 * 32 + 16);
#pragma unroll
    for (int v = 0; v < 4; ++v) {
      const u32 p = lds0 + REG_B + v * SLAB + t * 336 + c8 * 32;
      s0 += LD(f32x4, p); s1 += LD(f32x4, p + 16);
    }
    const u32x4 ga = LD(u32x4, lds0 + LDS_GA + t * 160 + c8 * 16);
    const float om = LD(float, lds0 + LDS_OMG + t * 4);
    u32x4 o;
#pragma unroll
    for (int e = 0; e < 4; ++e) {
      const float x0 = e < 2 ? s0[2 * e] : s1[2 * e - 4], x1 = e < 2 ? s0[2 * e + 1] : s1[2 * e - 3];
      o[e] = pk_bf16(bflo(ga[e]) + om * x0, bfhi(ga[e]) + om * x1);
    }
    *(u32x4*)(P.out + (size_t)(64 * m64 + t) * D + 80 * n80 + 8 * c8) = o;
  };
  if (wave < 2) { fin(tid); fin(tid + 512); } else fin(tid);
  STAMP(15);
}

extern "C" void kernel_launch(void* const* d_in, const int* in_sizes, int n_in, void* d_out, int out_size,
                              void* d_ws, size_t ws_size, hipStream_t stream) {
  Params P;
  P.u = (const u16*)d_in[0]; P.lar = (const u16*)d_in[1]; P.aim = (const u16*)d_in[2]; P.cre = (const u16*)d_in[3];
  P.cim = (const u16*)d_in[4]; P.ldt = (const u16*)d_in[5]; P.dsk = (const u16*)d_in[6]; P.w1 = (const u16*)d_in[8];
  P.b1 = (const u16*)d_in[9]; P.w2 = (const u16*)d_in[10]; P.b2 = (const u16*)d_in[11]; P.gw = (const u16*)d_in[12];
  P.out = (u16*)d_out; P.ws = (char*)d_ws;
  (void)hipFuncSetAttribute((const void*)fused, hipFuncAttributeMaxDynamicSharedMemorySize, LDS_TOTAL);
  fused<<<256, 512, LDS_TOTAL, stream>>>(P);
}
```

```cpp
#include <hip/hip_runtime.h>
#include <hip/hip_bf16.h>

typedef unsigned short u16;
typedef unsigned int u32;
typedef unsigned long long u64;
typedef short s16x4 __attribute__((ext_vector_type(4)));
typedef __bf16 bf16x2 __attribute__((ext_vector_type(2)));
typedef __bf16 bf16x8 __attribute__((ext_vector_type(8)));
typedef _Float16 f16x2 __attribute__((ext_vector_type(2)));
typedef _Float16 f16x8 __attribute__((ext_vector_type(8)));
typedef float f32x2 __attribute__((ext_vector_type(2)));
typedef float f32x4 __attribute__((ext_vector_type(4)));
typedef u32 u32x2 __attribute__((ext_vector_type(2)));
typedef int i32x4 __attribute__((ext_vector_type(4)));
typedef u32 u32x4 __attribute__((ext_vector_type(4)));
typedef __amdgpu_buffer_rsrc_t rsrc_t;
#define LAS __attribute__((address_space(3)))
#define GAS __attribute__((address_space(1)))
#define RLX_AGENT __ATOMIC_RELAXED, __HIP_MEMORY_SCOPE_AGENT
#define RLX_WG __ATOMIC_RELAXED, __HIP_MEMORY_SCOPE_WORKGROUP
#define DEV __device__ __forceinline__
#define HSLEEP 40
#define G2PRIO 1
#define CVPRIO 2
#define KTPRIO 0
#define G2DP 3
#define YGATE 8
#define YPS 4
#define HWAVE 0
#define INH 1
#define INY 0
#define HPS 4
#define SCONS 1
#define STLATE 1
#define STAG 0
#define SB() __builtin_amdgcn_sched_barrier(0)

constexpr int L = 2048, D = 640, NM = 128, F = 1728;
constexpr u32 WS_FLAGK = 2048;
constexpr u32 WS_FLAGW = 3072;
constexpr u32 WS_FLAGS = 4096;
constexpr u32 WS_FLAGH = WS_FLAGS + 2048;
constexpr u32 WS_FLAGY = WS_FLAGH + 2048;
constexpr u32 WS_INH = 16384;
constexpr u32 WS_INY = 49152;
constexpr u32 WS_GATE = 114688;
constexpr u32 WS_KX = 131072;
constexpr u32 WS_UT = WS_KX + 128 * 4096;
constexpr u32 WS_YT = WS_UT + D * L * 2;
constexpr u32 WS_HT = WS_YT + (D + 128) * L * 2;
constexpr u32 WS_W2T = WS_HT + 32 * 54 * 4096;
constexpr u32 WS_END = WS_W2T + 8 * 54 * 5120;
#define STAMP(k)
constexpr u32 SCR_W = 15616;
constexpr u32 REG_A = 0, REG_B = 65536;
constexpr u32 SLAB = 64 * 336;
constexpr u32 LDS_SIDE = 131072;
constexpr u32 LDS_X1 = REG_B + 24576, LDS_X2 = REG_B + 40960;
constexpr u32 LDS_GW = LDS_SIDE + 20480;
constexpr u32 LDS_B1 = LDS_GW + 2048;
constexpr u32 LDS_PAR = LDS_B1 + 1024;
constexpr u32 LDS_GA = LDS_GW;
constexpr u32 LDS_OMG = LDS_GA + 10240;
constexpr u32 LDS_B2 = LDS_OMG + 256;
constexpr u32 LDS_CTL = LDS_B2 + 320;
constexpr u32 LDS_TOTAL = LDS_CTL + 64;
#define DBG(i)
constexpr u32 CH_KC0 = 0, CH_K = 2576, CH_U = CH_K + 4608, UCP = 4160, CH_KC1 = 2 * SCR_W - 2576;
constexpr u32 LDS_YT = 0;
constexpr u32 CT_PAIR = LDS_CTL, CT_H = LDS_CTL + 16, CT_Y = LDS_CTL + 20, CT_R = LDS_CTL + 24, CT_FH = LDS_CTL + 28, CT_FY = LDS_CTL + 32, CT_FS = LDS_CTL + 36, CT_Q = LDS_CTL + 40, CT_A = LDS_CTL + 48, CT_S = LDS_CTL + 52, CT_FW = LDS_CTL + 56;

struct Params {
  const u16 *u, *lar, *aim, *cre, *cim, *ldt, *dsk, *w1, *b1, *w2, *b2, *gw;
  u16* out;
  char* ws;
};

DEV float bf2f(u32 x) { return __uint_as_float(x << 16); }
DEV float bflo(u32 x) { return __uint_as_float(x << 16); }
DEV float bfhi(u32 x) { return __uint_as_float(x & 0xffff0000u); }
DEV u32 pk_bf16(float a, float b) { f32x2 v = {a, b}; bf16x2 r = __builtin_convertvector(v, bf16x2); return __builtin_bit_cast(u32, r); }
DEV float rbf(float a) { return bflo(pk_bf16(a, a)); }
DEV u32 pk_f16(f32x2 v) { f16x2 r = __builtin_convertvector(v, f16x2); return __builtin_bit_cast(u32, r); }
DEV f32x2 cmul(f32x2 a, f32x2 b) {
  f32x2 t, r;
  asm("v_pk_mul_f32 %0, %1, %2 op_sel:[0,0] op_sel_hi:[0,1]" : "=v"(t) : "v"(a), "v"(b));
  asm("v_pk_fma_f32 %0, %1, %2, %3 op_sel:[1,1,0] op_sel_hi:[1,0,1] neg_lo:[1,0,0]" : "=v"(r) : "v"(a), "v"(b), "v"(t));
  return r;
}
DEV f32x2 cexp_p(float ar, float ai, float p) {
  float e = __builtin_amdgcn_exp2f(ar * p * 1.44269504088896341f);
  float t = ai * p;
  float hi = t * 0.15915494309189535f;
  float lo = __builtin_fmaf(t, 0.15915494309189535f, -hi);
  lo = __builtin_fmaf(t, 6.4206383e-9f, lo);
  float fr = (hi - __builtin_rintf(hi)) + lo;
  return (f32x2){e * __builtin_amdgcn_cosf(fr), e * __builtin_amdgcn_sinf(fr)};
}
#define WAIT_VM(n) asm volatile("s_waitcnt vmcnt(" #n ")" ::: "memory")
#define WAIT_LGKM0() asm volatile("s_waitcnt lgkmcnt(0)" ::: "memory")
DEV void bar() { asm volatile("s_waitcnt lgkmcnt(0)" ::: "memory"); __builtin_amdgcn_s_barrier(); asm volatile("" ::: "memory"); }
DEV void rawbar() { asm volatile("" ::: "memory"); __builtin_amdgcn_s_barrier(); asm volatile("" ::: "memory"); }

DEV void glds1(const void* base, u32 voff, u32 lds) {
  u32 keep;
  asm volatile("s_mov_b32 %0, m0\n\ts_mov_b32 m0, %3\n\ts_nop 0\n\tglobal_load_lds_dwordx4 %1, %2\n\ts_mov_b32 m0, %0"
               : "=&s"(keep) : "v"(voff), "s"(base), "s"(lds) : "memory");
}
DEV void glds1p(const void* p, u32 lds) {
  u32 keep;
  asm volatile("s_mov_b32 %0, m0\n\ts_mov_b32 m0, %2\n\ts_nop 0\n\tglobal_load_lds_dwordx4 %1, off\n\ts_mov_b32 m0, %0"
               : "=&s"(keep) : "v"(p), "s"(lds) : "memory");
}
DEV void glds4(rsrc_t rs, u32 v0, u32 v1, u32 v2, u32 v3, u32 s, u32 step, u32 lds) {
  u32 keep;
  asm volatile("s_mov_b32 %0, m0\n\ts_mov_b32 m0, %10\n\ts_nop 0\n\t"
               "buffer_load_dwordx4 %1, %5, %6 offen lds\n\t"
               "buffer_load_dwordx4 %2, %5, %7 offen offset:1024 lds\n\t"
               "buffer_load_dwordx4 %3, %5, %8 offen offset:2048 lds\n\t"
               "buffer_load_dwordx4 %4, %5, %9 offen offset:3072 lds\n\t"
               "s_mov_b32 m0, %0"
               : "=&s"(keep) : "v"(v0), "v"(v1), "v"(v2), "v"(v3), "s"(rs), "s"(s), "s"(s + step),
                 "s"(s + 2 * step), "s"(s + 3 * step), "s"(lds) : "memory");
}
DEV u32x2 tr_read(u32 a) {
  s16x4 r = __builtin_amdgcn_ds_read_tr16_b64_v4i16((LAS s16x4*)(size_t)a);
  return __builtin_bit_cast(u32x2, r);
}
DEV bf16x8 mk8(u32x2 a, u32x2 b) { u32x4 v = {a.x, a.y, b.x, b.y}; return __builtin_bit_cast(bf16x8, v); }
DEV f32x4 mma(bf16x8 a, bf16x8 b, f32x4 c) { return __builtin_amdgcn_mfma_f32_16x16x32_bf16(a, b, c, 0, 0, 0); }
#define LD(T, a) (*(const LAS T*)(size_t)(a))
#define ST(T, a) (*(LAS T*)(size_t)(a))

template <int SLEEP>
DEV void poll_flags(const char* p, u64 tag) {
#pragma unroll 1
  for (u32 spins = 0; spins < (1u << 18); ++spins) {
    u64 v = __hip_atomic_load((const GAS u64*)p, RLX_AGENT);
    if (__all(v == tag)) break;
    __builtin_amdgcn_s_sleep(SLEEP);
  }
  __builtin_amdgcn_fence(__ATOMIC_ACQUIRE, "wavefront");
  asm volatile("" ::: "memory");
}
DEV void put_flag(char* p, u64 tag) { __hip_atomic_store((GAS u64*)p, tag, RLX_AGENT); }
DEV u32 lds_arrive(u32 a, int lane) {
  WAIT_LGKM0();
  u32 r = 0;
  if (lane == 0) r = __hip_atomic_fetch_add((LAS u32*)(size_t)a, 1u, RLX_WG);
  return __builtin_amdgcn_readfirstlane(r);
}
DEV void lds_post(u32 a, int lane) {
  asm volatile("" ::: "memory");
  if (lane == 0) __hip_atomic_fetch_add((LAS u32*)(size_t)a, 1u, RLX_WG);
}
DEV void lds_wait(u32 a, u32 target) {
#pragma unroll 1
  for (u32 spins = 0; spins < (1u << 22); ++spins) {
    u32 v = __builtin_amdgcn_readfirstlane(__hip_atomic_load((LAS u32*)(size_t)a, RLX_WG));
    if (v >= target) break;
    __builtin_amdgcn_s_sleep(1);
  }
  asm volatile("" ::: "memory");
}

DEV void kgen(u32 pp, float ldt, u32 (&pk)[57], float& kscale) {
  const float dt = rbf(expf(ldt));
  const float ar = -rbf(expf(bf2f(LD(u16, pp)))) * dt, ai = bf2f(LD(u16, pp + 128)) * dt;
  const float cr = 2.f * bf2f(LD(u16, pp + 256)) * dt, ci = 2.f * bf2f(LD(u16, pp + 384)) * dt;
  u32 mx = max(__float_as_uint(cr) & 0x7fffffffu, __float_as_uint(ci) & 0x7fffffffu);
  mx = max(mx, (u32)__builtin_amdgcn_update_dpp(0, mx, 0x128, 0xf, 0xf, false));
  mx = max(mx, (u32)__builtin_amdgcn_update_dpp(0, mx, 0x124, 0xf, 0xf, false));
  mx = max(mx, (u32)__builtin_amdgcn_update_dpp(0, mx, 0x122, 0xf, 0xf, false));
  mx = max(mx, (u32)__builtin_amdgcn_update_dpp(0, mx, 0x121, 0xf, 0xf, false));
  u32 eb = max(max((u32)__builtin_amdgcn_readlane(mx, 0), (u32)__builtin_amdgcn_readlane(mx, 16)),
               max((u32)__builtin_amdgcn_readlane(mx, 32), (u32)__builtin_amdgcn_readlane(mx, 48))) >> 23;
  eb = eb < 40u ? 40u : (eb > 230u ? 230u : eb);
  const float inv = __uint_as_float((127u + 127u + 12u - eb) << 23);
  kscale = __uint_as_float((eb - 12u - 14u) << 23);
  const f32x2 e1 = cexp_p(ar, ai, 1.f), e8 = cexp_p(ar, -ai, 8.f), e128 = cexp_p(ar, ai, 128.f);
  {
    f32x2 G[16];
    const f32x2 e16 = cmul(e8, e8), e32 = cmul(e16, e16), e64 = cmul(e32, e32);
    G[0] = (f32x2){cr * inv, -ci * inv};
    G[1] = cmul(G[0], e8);
#pragma unroll
    for (int j = 0; j < 2; ++j) G[2 + j] = cmul(G[j], e16);
#pragma unroll
    for (int j = 0; j < 4; ++j) G[4 + j] = cmul(G[j], e32);
#pragma unroll
    for (int j = 0; j < 8; ++j) G[8 + j] = cmul(G[j], e64);
#pragma unroll
    for (int j = 0; j < 16; ++j) pk[j] = pk_f16(G[j]);
  }
  f32x2 Pw[8], Q[17];
  const f32x2 e2 = cmul(e1, e1), e4 = cmul(e2, e2);
  Pw[1] = e1; Pw[2] = e2; Pw[3] = cmul(e1, e2); Pw[4] = e4; Pw[5] = cmul(e1, e4); Pw[6] = cmul(e2, e4); Pw[7] = cmul(Pw[3], e4);
  const f32x2 e256 = cmul(e128, e128), e512 = cmul(e256, e256), e1024 = cmul(e512, e512);
  Q[1] = e128 * 16384.f; Q[2] = e256 * 16384.f; Q[4] = e512 * 16384.f; Q[8] = e1024 * 16384.f;
  Q[3] = cmul(Q[1], e256);
  Q[5] = cmul(Q[1], e512); Q[6] = cmul(Q[2], e512); Q[7] = cmul(Q[3], e512);
#pragma unroll
  for (int j = 1; j < 9; ++j) Q[8 + j] = cmul(Q[j], e1024);
  pk[16] = 0x00007400u;
#pragma unroll
  for (int j = 1; j < 8; ++j) pk[16 + j] = pk_f16(Pw[j] * 16384.f);
  pk[24] = pk_f16(Q[1]);
#pragma unroll
  for (int j = 1; j < 8; ++j) pk[24 + j] = pk_f16(cmul(Q[1], Pw[j]));
#pragma unroll
  for (int i = 2; i < 4; ++i) {
    pk[32 + 4 * (i - 2)] = pk_f16(Q[i]);
#pragma unroll
    for (int j = 1; j < 4; ++j) pk[32 + 4 * (i - 2) + j] = pk_f16(cmul(Q[i], Pw[2 * j]));
  }
#pragma unroll
  for (int i = 4; i < 8; ++i) { pk[40 + 2 * (i - 4)] = pk_f16(Q[i]); pk[41 + 2 * (i - 4)] = pk_f16(cmul(Q[i], Pw[4])); }
#pragma unroll
  for (int i = 8; i < 17; ++i) pk[48 + i - 8] = pk_f16(Q[i]);
}
template <int OFF>
DEV void put_rows(u32 base, const u32 (&pk)[57]) {
  u32 keep;
  asm volatile("s_mov_b32 %0, m0\n\ts_mov_b32 m0, %20\n\ts_nop 0\n\t"
               "ds_write_addtid_b32 %1 offset:%c21+0\n\t"
               "ds_write_addtid_b32 %2 offset:%c21+272\n\t"
               "ds_write_addtid_b32 %3 offset:%c21+544\n\t"
               "ds_write_addtid_b32 %4 offset:%c21+816\n\t"
               "ds_write_addtid_b32 %5 offset:%c21+1088\n\t"
               "ds_write_addtid_b32 %6 offset:%c21+1360\n\t"
               "ds_write_addtid_b32 %7 offset:%c21+1632\n\t"
               "ds_write_addtid_b32 %8 offset:%c21+1904\n\t"
               "ds_write_addtid_b32 %9 offset:%c21+2176\n\t"
               "ds_write_addtid_b32 %10 offset:%c21+2448\n\t"
               "ds_write_addtid_b32 %11 offset:%c21+2720\n\t"
               "ds_write_addtid_b32 %12 offset:%c21+2992\n\t"
               "ds_write_addtid_b32 %13 offset:%c21+3264\n\t"
               "ds_write_addtid_b32 %14 offset:%c21+3536\n\t"
               "ds_write_addtid_b32 %15 offset:%c21+3808\n\t"
               "ds_write_addtid_b32 %16 offset:%c21+4080\n\t"
               "ds_write_addtid_b32 %17 offset:%c21+4352\n\t"
               "ds_write_addtid_b32 %18 offset:%c21+4624\n\t"
               "ds_write_addtid_b32 %19 offset:%c21+4896\n\t"
               "s_mov_b32 m0, %0"
               : "=&s"(keep) : "v"(pk[0]), "v"(pk[1]), "v"(pk[2]), "v"(pk[3]), "v"(pk[4]), "v"(pk[5]), "v"(pk[6]), "v"(pk[7]), "v"(pk[8]), "v"(pk[9]), "v"(pk[10]), "v"(pk[11]), "v"(pk[12]), "v"(pk[13]), "v"(pk[14]), "v"(pk[15]), "v"(pk[16]), "v"(pk[17]), "v"(pk[18]), "s"(base), "i"(OFF) : "memory");
  asm volatile("s_mov_b32 %0, m0\n\ts_mov_b32 m0, %20\n\ts_nop 0\n\t"
               "ds_write_addtid_b32 %1 offset:%c21+5168\n\t"
               "ds_write_addtid_b32 %2 offset:%c21+5440\n\t"
               "ds_write_addtid_b32 %3 offset:%c21+5712\n\t"
               "ds_write_addtid_b32 %4 offset:%c21+5984\n\t"
               "ds_write_addtid_b32 %5 offset:%c21+6256\n\t"
               "ds_write_addtid_b32 %6 offset:%c21+6528\n\t"
               "ds_write_addtid_b32 %7 offset:%c21+6800\n\t"
               "ds_write_addtid_b32 %8 offset:%c21+7072\n\t"
               "ds_write_addtid_b32 %9 offset:%c21+7344\n\t"
               "ds_write_addtid_b32 %10 offset:%c21+7616\n\t"
               "ds_write_addtid_b32 %11 offset:%c21+7888\n\t"
               "ds_write_addtid_b32 %12 offset:%c21+8160\n\t"
               "ds_write_addtid_b32 %13 offset:%c21+8432\n\t"
               "ds_write_addtid_b32 %14 offset:%c21+8704\n\t"
               "ds_write_addtid_b32 %15 offset:%c21+8976\n\t"
               "ds_write_addtid_b32 %16 offset:%c21+9248\n\t"
               "ds_write_addtid_b32 %17 offset:%c21+9520\n\t"
               "ds_write_addtid_b32 %18 offset:%c21+9792\n\t"
               "ds_write_addtid_b32 %19 offset:%c21+10064\n\t"
               "s_mov_b32 m0, %0"
               : "=&s"(keep) : "v"(pk[19]), "v"(pk[20]), "v"(pk[21]), "v"(pk[22]), "v"(pk[23]), "v"(pk[24]), "v"(pk[25]), "v"(pk[26]), "v"(pk[27]), "v"(pk[28]), "v"(pk[29]), "v"(pk[30]), "v"(pk[31]), "v"(pk[32]), "v"(pk[33]), "v"(pk[34]), "v"(pk[35]), "v"(pk[36]), "v"(pk[37]), "s"(base), "i"(OFF) : "memory");
  asm volatile("s_mov_b32 %0, m0\n\ts_mov_b32 m0, %20\n\ts_nop 0\n\t"
               "ds_write_addtid_b32 %1 offset:%c21+10336\n\t"
               "ds_write_addtid_b32 %2 offset:%c21+10608\n\t"
               "ds_write_addtid_b32 %3 offset:%c21+10880\n\t"
               "ds_write_addtid_b32 %4 offset:%c21+11152\n\t"
               "ds_write_addtid_b32 %5 offset:%c21+11424\n\t"
               "ds_write_addtid_b32 %6 offset:%c21+11696\n\t"
               "ds_write_addtid_b32 %7 offset:%c21+11968\n\t"
               "ds_write_addtid_b32 %8 offset:%c21+12240\n\t"
               "ds_write_addtid_b32 %9 offset:%c21+12512\n\t"
               "ds_write_addtid_b32 %10 offset:%c21+12784\n\t"
               "ds_write_addtid_b32 %11 offset:%c21+13056\n\t"
               "ds_write_addtid_b32 %12 offset:%c21+13328\n\t"
               "ds_write_addtid_b32 %13 offset:%c21+13600\n\t"
               "ds_write_addtid_b32 %14 offset:%c21+13872\n\t"
               "ds_write_addtid_b32 %15 offset:%c21+14144\n\t"
               "ds_write_addtid_b32 %16 offset:%c21+14416\n\t"
               "ds_write_addtid_b32 %17 offset:%c21+14688\n\t"
               "ds_write_addtid_b32 %18 offset:%c21+14960\n\t"
               "ds_write_addtid_b32 %19 offset:%c21+15232\n\t"
               "s_mov_b32 m0, %0"
               : "=&s"(keep) : "v"(pk[38]), "v"(pk[39]), "v"(pk[40]), "v"(pk[41]), "v"(pk[42]), "v"(pk[43]), "v"(pk[44]), "v"(pk[45]), "v"(pk[46]), "v"(pk[47]), "v"(pk[48]), "v"(pk[49]), "v"(pk[50]), "v"(pk[51]), "v"(pk[52]), "v"(pk[53]), "v"(pk[54]), "v"(pk[55]), "v"(pk[56]), "s"(base), "i"(OFF) : "memory");
}
template <bool ANY>
DEV void ktable(u32 scr, u32 kb, const u32 (&pk)[57], float kscale, int lane) {
  const int l15 = lane & 15, q4 = lane >> 4;
  if (!ANY || scr < 65536) put_rows<0>(scr, pk); else put_rows<49152>(scr - 49152, pk);
  asm volatile("" ::: "memory");
  {
    f32x4 kc[3];
#pragma unroll
    for (int T = 0; T < 3; ++T) kc[T] = (f32x4){0.f, 0.f, 0.f, 0.f};
    const u32 rp = scr + l15 * 272 + q4 * 16;
    const u32 rp2 = scr + (48 + (l15 > 8 ? 8 : l15)) * 272 + q4 * 16;
    f16x8 fr[4][4];
#pragma unroll
    for (int ks = 0; ks < 4; ++ks) {
      fr[ks][0] = LD(f16x8, rp + ks * 64); fr[ks][1] = LD(f16x8, rp + 16 * 272 + ks * 64);
      fr[ks][2] = LD(f16x8, rp + 32 * 272 + ks * 64); fr[ks][3] = LD(f16x8, rp2 + ks * 64);
    }
    SB();
#pragma unroll
    for (int ks = 0; ks < 4; ++ks)
#pragma unroll
      for (int T = 0; T < 3; ++T) kc[T] = __builtin_amdgcn_mfma_f32_16x16x32_f16(fr[ks][1 + T], fr[ks][0], kc[T], 0, 0, 0);
    WAIT_LGKM0();
    ST(f32x4, kb + (128 * (q4 >> 1) + 8 * l15 + 4 * (q4 & 1)) * 4) = kc[0] * kscale;
    const f32x4 v1 = kc[1] * kscale, v2 = kc[2] * kscale;
    if (q4 < 2) {
      ST(f32x4, kb + (256 + 64 * q4 + 4 * l15) * 4) = v1;
#pragma unroll
      for (int rr = 0; rr < 4; ++rr) ST(float, kb + (512 + 16 * (4 * q4 + rr) + l15) * 4) = v2[rr];
    } else {
      ST(f32x2, kb + (384 + 64 * (q4 - 2) + 2 * l15) * 4) = (f32x2){v1[0], v1[1]};
      ST(f32x2, kb + (416 + 64 * (q4 - 2) + 2 * l15) * 4) = (f32x2){v1[2], v1[3]};
      if (lane == 32) ST(float, kb + 640 * 4) = v2[0];
    }
  }
}
DEV void expand(u32 chb, int half, int lane, u32x4& c0, u32x4& c1) {
  {
    auto tab = [&](int i) { return LD(float, chb + CH_KC0 + i * 4) + LD(float, chb + CH_KC1 + i * 4); };
    const int m = 255 - 64 * half - lane;
    const float a = tab(512 + m - 127), b = tab(512 + m - 128), c = (m & 1) ? a : b;
    const u32 bb = pk_bf16(b, b);
    c0 = (u32x4){pk_bf16(a, a), pk_bf16(a, c), bb, bb};
    if (half == 0) {
      const int i = 384 + 2 * (63 - lane);
      const float x = tab(i + 2), y = tab(i + 1), z = tab(i);
      c1 = (u32x4){pk_bf16(x, x), pk_bf16(y, y), pk_bf16(y, z), pk_bf16(z, z)};
    } else {
      const bool lo = lane >= 32;
      const int i = lo ? 8 * (63 - lane) : 256 + 4 * (31 - lane);
      const f32x4 s0 = LD(f32x4, chb + CH_KC0 + i * 4) + LD(f32x4, chb + CH_KC1 + i * 4);
      const f32x4 s1 = LD(f32x4, chb + CH_KC0 + i * 4 + 16) + LD(f32x4, chb + CH_KC1 + i * 4 + 16);
      c1 = (u32x4){pk_bf16(lo ? s1[3] : s1[0], lo ? s1[2] : s0[3]), pk_bf16(lo ? s1[1] : s0[2], lo ? s1[0] : s0[2]),
                   pk_bf16(lo ? s0[3] : s0[2], lo ? s0[2] : s0[1]), pk_bf16(lo ? s0[1] : s0[0], s0[0])};
    }
  }
}

typedef u32x2 __attribute__((aligned(4))) u32x2a;
DEV u32x2 ld2(u32 a) { return *(const LAS u32x2a*)(size_t)a; }
struct ConvFrags { bf16x8 A[2][8], B[2][8]; };
template <bool NEXT>
DEV void conv_step(ConvFrags& f, int set, u32 pa, u32 pa8, u32 pb, f32x4 (&acc)[8]) {
  u32x2 lo[8], hi[8];
#pragma unroll
  for (int g = 0; g < 12; ++g) {
    if (NEXT) {
#pragma unroll
      for (int d = 2 * g; d < 2 * g + 2; ++d) {
        const int k = d / 3;
        if (d % 3 == 0) lo[k] = ld2(pa + 512 * k);
        if (d % 3 == 1) { hi[k] = ld2(pa8 + 512 * k); f.A[set ^ 1][k] = mk8(lo[k], hi[k]); }
        if (d % 3 == 2) f.B[set ^ 1][7 - k] = LD(bf16x8, pb + 512 * k);
      }
    }
#pragma unroll
    for (int j = 3 * g; j < 3 * g + 3; ++j) {
      const int k = j < 1 ? 0 : j < 3 ? 1 : j < 6 ? 2 : j < 10 ? 3 : j < 15 ? 4 : j < 21 ? 5 : j < 28 ? 6 : 7, e = j - k * (k + 1) / 2;
      acc[e + 7 - k] = mma(f.A[set][e], f.B[set][7 - k], acc[e + 7 - k]);
    }
    if (NEXT) {
#pragma unroll
      for (int j = 0; j < 2; ++j) { __builtin_amdgcn_sched_group_barrier(0x8, 1, 0); __builtin_amdgcn_sched_group_barrier(0x100, 1, 0); }
      __builtin_amdgcn_sched_group_barrier(0x8, 1, 0);
    }
    SB();
  }
}

__global__ __launch_bounds__(512, 2) void fused(Params P) {
  extern __shared__ __attribute__((aligned(16))) char smem[];
  const int tid = threadIdx.x, lane = tid & 63;
  const int wave = __builtin_amdgcn_readfirstlane(tid >> 6);
  const int bid = blockIdx.x, xcd = bid & 7, r = bid >> 3;
  const bool regular = r < 28;
  const int quad = (wave >> 2) ^ 1, qw = wave & 3, half = qw & 1;
  const int l15 = lane & 15, q4 = lane >> 4;
  const u32 lds0 = (u32)(size_t)(LAS char*)smem;
  char* ws = P.ws;
  const int rq = (r * 37) >> 8, nl = r - 7 * rq, mi = (xcd >> 1) * 4 + rq, ni = (xcd & 1) * 7 + nl;
  const int rot = nl + 7 * rq - 10 * ((nl + 7 * rq) / 10);
  const int m64 = (xcd >> 1) * 8 + (r >> 2), n80 = (xcd & 1) * 4 + (r & 3);
  const rsrc_t wsr = __builtin_amdgcn_make_buffer_rsrc(ws, 0, WS_END, 0x00020000);
  const int chan0 = 80 * xcd + 2 * r;
  const u32 chb = lds0 + REG_A + (qw >> 1) * (2 * SCR_W);
  const int ch = chan0 + (qw >> 1);
  u64 tag;
  const char* hfl = INH ? ws + WS_INH + (m64 * 8 + n80) * 128 : ws + WS_FLAGH + (m64 >> 1) * 128;
  const char* yfl = INY ? ws + WS_INY + (m64 * 8 + n80) * 256 : ws + WS_FLAGY + n80 * 256;

  auto par_load = [&](int ch, u32 dst) {
    const int a = (lane >> 3) & 3;
    const u16 *p0 = P.lar, *p1 = P.aim, *p2 = P.cre, *p3 = P.cim;
    asm volatile("" : "+s"(p0), "+s"(p1), "+s"(p2), "+s"(p3));
    const u16* p = a == 0 ? p0 : a == 1 ? p1 : a == 2 ? p2 : p3;
    if (lane < 32) glds1p(p + ch * NM + 64 * half + (lane & 7) * 8, dst);
  };
  const bool isw = false, side = qw < 2;
  auto side_load = [&]() {
#pragma unroll
    for (int i = 0; i < 5; ++i) {
      const int o = i * 64 + lane, row = (o * 6554) >> 16, c = o - 10 * row;
      glds1(isw ? P.w2 : P.u, (u32)((m64 * 64 + half * 32 + row) * (D * 2) + n80 * 160 + c * 16), lds0 + LDS_SIDE + qw * 5120 + i * 1024);
    }
  };
  u32x4 sv[5];
  auto side_read = [&]() {
    const u32 p = lds0 + LDS_SIDE + qw * 5120 + ((isw ? 4 : 8) * q4 + (l15 >> 2)) * 160 + 8 * (lane & 3), p2 = p + (isw ? 16 * 160 : 4 * 160);
#pragma unroll
    for (int i = 0; i < 5; ++i) { const u32x2 a = tr_read(p + 32 * i), b = tr_read(p2 + 32 * i); sv[i] = (u32x4){a.x, a.y, b.x, b.y}; }
  };
  auto side_store = [&]() {
    const u32 o = isw ? WS_W2T + (u32)((n80 * 54 + 2 * m64 + half) * 5120) + lane * 16
                      : WS_UT + (u32)((80 * n80 + l15) * (L * 2) + (64 * m64 + 32 * half) * 2 + q4 * 16);
#pragma unroll
    for (int i = 0; i < 5; ++i) __builtin_amdgcn_raw_buffer_store_b128(sv[i], wsr, o + i * (isw ? 1024 : 16 * L * 2), 0, 16);
  };

  u32 ldt2 = 0;
  if (!regular) {
    const int xi = 4 * (r - 28) + 2 * quad + (qw >> 1);
    par_load(80 * xcd + 64 + xi, lds0 + LDS_PAR + (4 + wave) * 512);
    if (quad == 0) par_load(chan0 + (qw >> 1), lds0 + LDS_PAR + qw * 512);
    if (quad == 1 && side) side_load();
    if (tid < 16) ST(u32, lds0 + LDS_CTL + tid * 4) = 0;
    u32 ldx;
    asm volatile("s_load_dwordx2 %0, %3, 0x0\n\ts_load_dword %1, %4, 0x0\n\ts_load_dword %2, %5, 0x0\n\ts_waitcnt lgkmcnt(0)"
                 : "=&s"(tag), "=&s"(ldt2), "=&s"(ldx) : "s"(ws), "s"(P.ldt + chan0), "s"(P.ldt + 80 * xcd + 64 + (xi & ~1)) : "memory");
    tag += 1;
    if (quad == 1 && side) WAIT_VM(5); else WAIT_VM(0);
    u32 pk[57];
    float kscale;
    kgen(lds0 + LDS_PAR + (4 + wave) * 512 + lane * 2, (xi & 1) ? bfhi(ldx) : bflo(ldx), pk, kscale);
#pragma unroll
    for (int j = 0; j < 57; ++j) asm volatile("" : "+v"(pk[j]));
    WAIT_VM(0);
    STAMP(1);
    if (quad == 1 && side) { side_read(); side_store(); }
    const u32 xb = chb + (quad ? REG_B : 0);
    ktable<true>(xb + half * SCR_W, xb + (half ? CH_KC1 : CH_KC0), pk, kscale, lane);
    bar();
    u32x4 c0, c1;
    expand(xb, half, lane, c0, c1);
    const u32 ko = WS_KX + (u32)((xcd * 16 + xi) * 4096 + half * 1024) + lane * 16;
    __builtin_amdgcn_raw_buffer_store_b128(c0, wsr, ko, 0, 16);
    __builtin_amdgcn_raw_buffer_store_b128(c1, wsr, ko + 2048, 0, 16);
    WAIT_VM(0);
    bar();
    if (tid == 0) { put_flag(ws + WS_FLAGK + (xcd * 4 + r - 28) * 8, tag); put_flag(ws + WS_FLAGS + (n80 * 32 + m64) * 8, tag); }
    STAMP(2);
    if (quad == 1) {
      const int kb0 = 7 * (r - 28) + qw, nb = (qw < 3 && kb0 + 4 < 27) ? 2 : 1;
#pragma unroll 1
      for (int b = 0; b < nb; ++b)
#pragma unroll
        for (int i = 0; i < 10; ++i) {
          const int o = i * 64 + lane, row = (o * 6554) >> 16, c = o - 10 * row;
          glds1(P.w2, (u32)(((kb0 + 4 * b) * 64 + row) * (D * 2) + xcd * 160 + c * 16), lds0 + REG_B + (2 * qw + b) * 10240 + i * 1024);
        }
      WAIT_VM(0);
#pragma unroll 1
      for (int hf = 0; hf < 2 * nb; ++hf) {
        const u32 p = lds0 + REG_B + 2 * qw * 10240 + hf * 5120 + (4 * q4 + (l15 >> 2)) * 160 + 8 * (lane & 3);
        u32x2 a[5], b[5];
#pragma unroll
        for (int i = 0; i < 5; ++i) { a[i] = tr_read(p + 32 * i); b[i] = tr_read(p + 16 * 160 + 32 * i); }
#pragma unroll
        for (int i = 0; i < 5; ++i)
          __builtin_amdgcn_raw_buffer_store_b128((u32x4){a[i].x, a[i].y, b[i].x, b[i].y}, wsr,
              WS_W2T + (u32)((xcd * 54 + 2 * (kb0 + 4 * (hf >> 1)) + (hf & 1)) * 5120 + i * 1024) + lane * 16, 0, 16);
      }
      WAIT_VM(0);
      if (lds_arrive(lds0 + CT_R, lane) == 3 && lane == 0) put_flag(ws + WS_FLAGW + (xcd * 4 + r - 28) * 8, tag);
      STAMP(3);
    }
  }

  if (quad == 0) {
    const rsrc_t xr = __builtin_amdgcn_make_buffer_rsrc((void*)P.u, 0, L * D * 2, 0x00020000);
    const rsrc_t wr = __builtin_amdgcn_make_buffer_rsrc((void*)P.w1, 0, D * F * 2, 0x00020000);
    const u32 xv = (u32)((lane >> 3) * (D * 2) + ((lane & 7) ^ q4) * 16);
    const u32 wv = (u32)(q4 * (F * 2) + (l15 ^ (q4 << 2)) * 16);
    const u32 xs = (u32)((mi * 128 + 32 * qw) * (D * 2)), wso = (u32)(16 * qw * (F * 2) + ni * 256);
    auto stage = [&](int i) {
      const u32 dst = lds0 + ((i + 2) & 3) * 32768 + qw * 4096;
      const int k = i + rot >= 10 ? i + rot - 10 : i + rot;
      glds4(xr, xv, xv ^ 64, xv, xv ^ 64, xs + k * 128, 8 * D * 2 - 1024, dst);
      glds4(wr, wv, wv ^ 16, wv ^ 32, wv ^ 48, wso + k * (64 * F * 2), 4 * F * 2 - 1024, dst + 16384);
    };
    if (regular) {
      __builtin_amdgcn_s_setprio(3);
      rawbar();
      par_load(chan0 + (qw >> 1), lds0 + LDS_PAR + qw * 512);
      stage(0);
      if (qw == 0) glds1(P.b1, (u32)(ni * 256 + (lane & (ni == 13 ? 7 : 15)) * 16), lds0 + LDS_B1);
      if (qw == 1 && ni == 13) { glds1(P.gw, lane * 16, lds0 + LDS_GW); glds1(P.gw, 1024 + l15 * 16, lds0 + LDS_GW + 1024); }
      stage(1);
      asm volatile("s_load_dwordx2 %0, %2, 0x0\n\ts_load_dword %1, %3, 0x0\n\ts_waitcnt lgkmcnt(0)"
                   : "=&s"(tag), "=&s"(ldt2) : "s"(ws), "s"(P.ldt + chan0) : "memory");
      tag += 1;
      WAIT_VM(16);
    }
    u32 pk[57];
    float kscale;
    kgen(lds0 + LDS_PAR + qw * 512 + lane * 2, (qw >> 1) ? bfhi(ldt2) : bflo(ldt2), pk, kscale);
#pragma unroll
    for (int j = 0; j < 57; ++j) asm volatile("" : "+v"(pk[j]));
    STAMP(5);
    if (regular) {
      if (STLATE == 2) { WAIT_VM(8); if (lane == 0) __hip_atomic_fetch_add((LAS u32*)(size_t)(lds0 + CT_FS), 1u, RLX_WG); }
      stage(2);
#pragma unroll 1
      for (int s = 0; s < 7; ++s) {
        WAIT_VM(8);
        rawbar();
        stage(s + 3);
        if (!SCONS && s == 3 && tid == 256) put_flag(ws + WS_FLAGS + (n80 * 32 + m64) * 8, tag);
      }
      WAIT_VM(8);
      rawbar();
      WAIT_VM(0);
      rawbar();
    }
    STAMP(4);
    __builtin_amdgcn_s_setprio(KTPRIO);
    {
      const char* fp = lane < 32 ? ws + WS_FLAGS + (xcd * 32 + lane) * 8 : ws + WS_FLAGK + (xcd * 4 + (r >> 3)) * 8;
      const u64 f0 = __hip_atomic_load((const GAS u64*)fp, RLX_AGENT);
      ktable<false>(chb + half * SCR_W, chb + (half ? CH_KC1 : CH_KC0), pk, kscale, lane);
      if (!__all(f0 == tag)) {
        if (!regular) __builtin_amdgcn_s_sleep(HSLEEP);
        poll_flags<16>(fp, tag);
      }
      __builtin_amdgcn_fence(__ATOMIC_ACQUIRE, "wavefront");
      u32x4 ud[3], kx;
      u32 un[3];
      const u32 uo = WS_UT + (u32)(ch * (L * 2) + half * 2048);
#pragma unroll
      for (int i = 0; i < 2; ++i) {
        ud[i] = __builtin_amdgcn_raw_buffer_load_b128(wsr, lane * 16 + i * 1024, uo, 16);
        un[i] = __builtin_amdgcn_raw_buffer_load_b32(wsr, lane * 16 + i * 1024 + 16, uo, 16);
      }
      const u32 xo = WS_UT + (u32)((80 * xcd + 64 + (r >> 1)) * (L * 2) + qw * 1024);
      ud[2] = __builtin_amdgcn_raw_buffer_load_b128(wsr, lane * 16, xo, 16);
      un[2] = __builtin_amdgcn_raw_buffer_load_b32(wsr, lane * 16 + 16, xo, 16);
      kx = __builtin_amdgcn_raw_buffer_load_b128(wsr, lane * 16, WS_KX + (u32)((xcd * 16 + (r >> 1)) * 4096 + qw * 1024), 16);
      STAMP(1);
      lds_post(lds0 + CT_PAIR + (qw >> 1) * 4, lane);
      lds_wait(lds0 + CT_PAIR + (qw >> 1) * 4, 2);
      u32x4 c0, c1;
      expand(chb, half, lane, c0, c1);
      ST(u32x4, chb + CH_K + (64 * half + lane) * 16) = c0;
      ST(u32x4, chb + CH_K + (128 + 64 * half + lane) * 16) = c1;
      auto put2 = [&](u32 a, u32x4 d, u32 n) {
        ST(u32x4, a) = d;
        ST(u32x4, a + UCP) = (u32x4){__builtin_amdgcn_alignbit(d[1], d[0], 16), __builtin_amdgcn_alignbit(d[2], d[1], 16),
                                     __builtin_amdgcn_alignbit(d[3], d[2], 16), __builtin_amdgcn_alignbit(n, d[3], 16)};
      };
      auto edges = [&](u32 blk, u32x4 d) {
        const u32x4 z = {0, 0, 0, 0};
        if (lane < 2) put2(blk + CH_U + 16 - lane * 16, z, lane ? 0 : d[0]);
        else if (lane < 32) ST(u32x4, blk + CH_K + (254 + lane) * 16) = z;
      };
#pragma unroll
      for (int i = 0; i < 2; ++i) put2(chb + CH_U + 32 + half * 2048 + i * 1024 + lane * 16, ud[i], un[i]);
      if (half == 0) edges(chb, ud[0]);
      lds_wait(lds0 + CT_R, 4u);
      put2(lds0 + REG_B + CH_U + 32 + qw * 1024 + lane * 16, ud[2], un[2]);
      ST(u32x4, lds0 + REG_B + CH_K + qw * 1024 + lane * 16) = kx;
      if (qw == 0) edges(lds0 + REG_B, ud[2]);
    }
    lds_post(lds0 + CT_Q, lane);
    STAMP(8);
    __builtin_amdgcn_s_setprio(G2PRIO);
    if (regular) lds_wait(lds0 + CT_R, 4u);
    constexpr int NS = 14, DP = G2DP;
    const int first = qw >> 1;
    const int k0 = (qw < 2 ? 14 * qw : 28 + 13 * (qw - 2)) - first;
    const u32 hb = WS_HT + (u32)((m64 * 54 + k0) * 4096), wb = WS_W2T + (u32)((n80 * 54 + k0) * 5120);
    const u32 lo = lane * 16;
    u32x4 fr[DP][9];
    auto load1 = [&](int s, int i) {
      const int j = s;
      fr[s % DP][i] = i < 5 ? __builtin_amdgcn_raw_buffer_load_b128(wsr, lo + (i & 3) * 1024, wb + j * 5120 + (i >> 2) * 4096, 16)
                            : __builtin_amdgcn_raw_buffer_load_b128(wsr, lo + (i - 5) * 1024, hb + j * 4096, 16);
    };
    STAMP(6);
    if (qw == 0) {
      if (regular) lds_wait(lds0 + CT_H, 4u);
      poll_flags<HPS>(lane < 14 ? hfl + lane * 8 : ws + WS_FLAGW + (n80 * 4 + (lane & 3)) * 8, tag);
      if (lane == 0) __hip_atomic_store((LAS u32*)(size_t)(lds0 + CT_FH), 1u, RLX_WG);
    } else {
      lds_wait(lds0 + CT_FH, 1u);
    }
    STAMP(11);
#pragma unroll
    for (int s = 0; s < DP - 1; ++s)
      if (s >= first) {
#pragma unroll
        for (int i = 0; i < 9; ++i) load1(s, i);
      }
    f32x4 acc[5][4];
#pragma unroll
    for (int a = 0; a < 5; ++a)
#pragma unroll
      for (int b = 0; b < 4; ++b) acc[a][b] = (f32x4){0.f, 0.f, 0.f, 0.f};
    SB();
#pragma unroll
    for (int s = 0; s < NS; ++s) {
      if (s == YGATE && lane == 0) __hip_atomic_fetch_add((LAS u32*)(size_t)(lds0 + CT_FW), 1u, RLX_WG);
      if (s >= (STAG ? 3 : 1) || s >= first) {
#pragma unroll
        for (int a = 0; a < 5; ++a) {
          if (s + DP - 1 < NS) { load1(s + DP - 1, 2 * a); if (a < 4) load1(s + DP - 1, 2 * a + 1); }
#pragma unroll
          for (int b = 0; b < 4; ++b)
            acc[a][b] = mma(__builtin_bit_cast(bf16x8, fr[s % DP][a]), __builtin_bit_cast(bf16x8, fr[s % DP][5 + b]), acc[a][b]);
          SB();
        }
      } else {
        if (s + DP - 1 >= first) {
#pragma unroll
          for (int i = 0; i < 9; ++i) load1(s + DP - 1, i);
        }
        SB();
      }
    }
    STAMP(12);
    lds_wait(lds0 + CT_Y, 4u);
    const u32 sl = lds0 + REG_B + qw * SLAB + l15 * 336 + q4 * 16;
#pragma unroll
    for (int a = 0; a < 5; ++a)
#pragma unroll
      for (int b = 0; b < 4; ++b) ST(f32x4, sl + b * 16 * 336 + a * 64) = acc[a][b];
  } else {
    const bool lastn = ni == 13;
    u32x4 uu[3], dd[3];
    u32 b2v;
    auto ud_load = [&]() {
#pragma unroll
      for (int i = 0; i < 3; ++i) {
        const int c = 4 * i + q4 < 10 ? 4 * i + q4 : 9;
        uu[i] = *(const u32x4*)(P.u + (size_t)(64 * m64 + 16 * qw + l15) * D + 80 * n80 + 8 * c);
        dd[i] = *(const u32x4*)(P.dsk + 80 * n80 + 8 * c);
      }
      b2v = *(const u32*)(P.b2 + 80 * n80 + (tid < 80 ? tid & ~1 : 0));
    };
    auto ud_fence = [&]() { asm volatile("" : "+v"(uu[0]), "+v"(uu[1]), "+v"(uu[2]), "+v"(dd[0]), "+v"(dd[1]), "+v"(dd[2]), "+v"(b2v)); };
    if (!regular) { ud_load(); ud_fence(); }
    if (regular) {
      if (side) side_load();
      if (tid < 16) ST(u32, lds0 + LDS_CTL + tid * 4) = 0;
      bar();
      asm volatile("s_load_dwordx2 %0, %1, 0x0\n\ts_waitcnt lgkmcnt(0)" : "=&s"(tag) : "s"(ws) : "memory");
      tag += 1;
      WAIT_VM(0);
      STAMP(5);
      side_read();
      STAMP(6);
      __builtin_amdgcn_s_setprio(2);
      const int wf = qw & 1, wt = qw >> 1;
      u32 xa[2], wa[4][2];
      xa[0] = lds0 + 65536 + (u32)((64 * wt + l15) * 128 + ((q4 ^ (l15 >> 1)) * 16)); xa[1] = xa[0] ^ 64;
      f32x4 acc[4][4];
      if (!(lastn && wf)) {
#pragma unroll
        for (int t = 0; t < 2; ++t) {
          const int qq = l15 >> 2, p = lane & 3;
          const int row = 8 * q4 + 4 * t + qq, c = 8 * wf + (p >> 1);
          const u32 a0 = lds0 + 65536 + 16384u + 256u * row + 16u * (c ^ ((qq << 2) | ((2 * q4 + t) & 3))) + 8u * (p & 1);
#pragma unroll
          for (int ft = 0; ft < 4; ++ft) wa[ft][t] = a0 ^ (ft << 5);
        }
        bf16x8 A[2][4], B[2][4];
        auto rdf = [&](int set, int i, u32 so, int kk) {
          A[set][i] = mk8(tr_read(wa[i][0] + so + kk * 8192), tr_read(wa[i][1] + so + kk * 8192));
          B[set][i] = LD(bf16x8, xa[kk] + so + i * 2048);
        };
        auto hstep = [&](int set, u32 so, int kk) {
          constexpr int ord[16][2] = {{0, 0}, {0, 1}, {1, 0}, {1, 1}, {0, 2}, {1, 2}, {2, 0}, {2, 1},
                                      {2, 2}, {0, 3}, {1, 3}, {2, 3}, {3, 0}, {3, 1}, {3, 2}, {3, 3}};
#pragma unroll
          for (int g = 0; g < 4; ++g) {
            rdf(set ^ 1, g, so, kk);
#pragma unroll
            for (int j = 0; j < 4; ++j) {
              const int a = ord[4 * g + j][0], b = ord[4 * g + j][1];
              acc[a][b] = mma(A[set][a], B[set][b], acc[a][b]);
            }
#pragma unroll
            for (int j = 0; j < 3; ++j) { __builtin_amdgcn_sched_group_barrier(0x8, 1, 0); __builtin_amdgcn_sched_group_barrier(0x100, 1, 0); }
            __builtin_amdgcn_sched_group_barrier(0x8, 1, 0);
            SB();
          }
        };
        bar();
        STAMP(1);
#pragma unroll
        for (int i = 0; i < 4; ++i) rdf(0, i, 0, 0);
        if (STLATE == 1 && side) side_store();
#pragma unroll
        for (int ft = 0; ft < 4; ++ft) {
          const u32x2 bi = LD(u32x2, lds0 + LDS_B1 + (wf * 64 + ft * 16 + 4 * q4) * 2);
#pragma unroll
          for (int tt = 0; tt < 4; ++tt) acc[ft][tt] = (f32x4){bflo(bi.x), bfhi(bi.x), bflo(bi.y), bfhi(bi.y)};
        }
        auto flip = [&]() {
          xa[0] ^= 65536; xa[1] ^= 65536;
#pragma unroll
          for (int ft = 0; ft < 4; ++ft) { wa[ft][0] ^= 65536; wa[ft][1] ^= 65536; }
          SB();
        };
#pragma unroll 1
        for (int p = 0; p < 4; ++p) {
          hstep(0, 0, 1); hstep(1, 32768, 0);
          if (p == 1) WAIT_VM(0);
          rawbar();
          if (SCONS && p == 1 && tid == 0) put_flag(ws + WS_FLAGS + (n80 * 32 + m64) * 8, tag);
          SB();
          hstep(0, 32768, 1);
          flip();
          hstep(1, 0, 0);
          rawbar(); SB();
        }
        ud_load(); SB();
        hstep(0, 0, 1); hstep(1, 32768, 0); hstep(0, 32768, 1); hstep(1, 0, 0);
        ud_fence();
        STAMP(2);
        const u32 ho = WS_HT + (u32)(((2 * mi + wt) * 54 + 4 * ni + 2 * wf) * 4096);
#pragma unroll
        for (int kh = 0; kh < 2; ++kh)
#pragma unroll
          for (int tt = 0; tt < 4; ++tt) {
            u32x4 o;
#pragma unroll
            for (int h = 0; h < 2; ++h) {
              f32x4 v = __builtin_bit_cast(f32x4, __builtin_elementwise_max(__builtin_bit_cast(i32x4, acc[2 * kh + h][tt]), (i32x4){0, 0, 0, 0}));
              v = v * v;
              o[2 * h] = pk_bf16(v[0], v[1]); o[2 * h + 1] = pk_bf16(v[2], v[3]);
            }
            __builtin_amdgcn_raw_buffer_store_b128(o, wsr, ho + kh * 4096 + lane * 16 + tt * 1024, 0, 16);
          }
      } else {
#pragma unroll
        for (int tt = 0; tt < 4; ++tt) acc[0][tt] = (f32x4){0.f, 0.f, 0.f, 0.f};
#pragma unroll 1
        for (int s = 0; s < 10; ++s) {
          if (s == 3) WAIT_VM(0);
          if (s < 9) bar();
          if (STLATE == 1 && s == 0 && side) side_store();
          const int k = s + rot >= 10 ? s + rot - 10 : s + rot;
#pragma unroll
          for (int kk = 0; kk < 2; ++kk) {
            const bf16x8 Ag = LD(bf16x8, lds0 + LDS_GW + k * 128 + kk * 64 + q4 * 16);
#pragma unroll
            for (int tt = 0; tt < 4; ++tt) acc[0][tt] = mma(Ag, LD(bf16x8, (xa[kk] ^ (((s + 2) & 3) * 32768) ^ 65536) + tt * 2048), acc[0][tt]);
          }
        }
        ud_load(); ud_fence();
        float pre = q4 == 0 ? acc[0][0][0] : q4 == 1 ? acc[0][1][0] : q4 == 2 ? acc[0][2][0] : acc[0][3][0];
        pre = rbf(pre);
        const float g = rbf(1.f / (1.f + __expf(-pre)));
        __builtin_amdgcn_raw_buffer_store_b32(__float_as_uint(g), wsr, WS_GATE + (u32)(mi * 128 + 64 * wt + lane) * 4, 0, 16);
      }
      if (lane == 0) __hip_atomic_fetch_add((LAS u32*)(size_t)(lds0 + CT_R), 1u, RLX_WG);
      STAMP(3);
    }
    __builtin_amdgcn_s_setprio(CVPRIO);
    {
      if (regular) {
        lds_wait(lds0 + CT_R, 4u);
      } else {
        lds_wait(lds0 + CT_PAIR, 2); lds_wait(lds0 + CT_PAIR + 4, 2);
      }
#pragma unroll
      for (int i = 0; i < 3; ++i) {
        const int c = 4 * i + q4 < 10 ? 4 * i + q4 : 9;
        u32x4 v;
#pragma unroll
        for (int e = 0; e < 4; ++e) v[e] = pk_bf16(bflo(uu[i][e]) * bflo(dd[i][e]), bfhi(uu[i][e]) * bfhi(dd[i][e]));
        ST(u32x4, lds0 + LDS_GA + (16 * qw + l15) * 160 + c * 16) = v;
      }
      if (tid < 80) ST(float, lds0 + LDS_B2 + tid * 4) = (tid & 1) ? bfhi(b2v) : bflo(b2v);
    }
    if (regular) {
      WAIT_VM(0);
      if (lds_arrive(lds0 + CT_H, lane) == 3 && lane < 16) put_flag(ws + WS_INH + ((mi * 16 + lane) * 16 + ni) * 8, tag);
      STAMP(4);
    }
    lds_wait(lds0 + CT_Q, 4u);
    STAMP(8);
    {
      f32x4 acc[8], acx[8];
#pragma unroll
      for (int j = 0; j < 8; ++j) { acc[j] = (f32x4){0.f, 0.f, 0.f, 0.f}; acx[j] = acc[j]; }
      const int z = l15 + 8 * q4 + 1;
      const u32 oa = CH_U + (z & 1) * UCP + (z & ~1) * 2, ob = CH_K + (2032 - 16 * l15 + 8 * q4) * 2 - 512 * 7;
      const u32 b0 = chb + half * 256, b1 = lds0 + REG_B + (4 * (r & 1) + qw) * 64;
      u32 pa = b0 + oa, pa8 = pa + 8, pb = b0 + ob, xa = b1 + oa, xa8 = xa + 8, xb = b1 + ob;
      asm volatile("" : "+v"(pa8), "+v"(xa8));
      ConvFrags f;
#pragma unroll
      for (int k = 0; k < 8; ++k) { f.A[0][k] = mk8(ld2(pa + 512 * k), ld2(pa8 + 512 * k)); f.B[0][7 - k] = LD(bf16x8, pb + 512 * k); }
      SB();
      conv_step<true>(f, 0, pa + 64, pa8 + 64, pb + 64, acc);
      conv_step<true>(f, 1, pa + 128, pa8 + 128, pb + 128, acc);
      conv_step<true>(f, 0, pa + 192, pa8 + 192, pb + 192, acc);
      conv_step<true>(f, 1, xa, xa8, xb, acc);
      conv_step<false>(f, 0, 0, 0, 0, acx);
      STAMP(9);
      if (half) {
#pragma unroll
        for (int j = 0; j < 4; ++j) { const f32x4 t = acc[j]; acc[j] = acc[4 + j]; acc[4 + j] = t; asm volatile("" : "+v"(acc[j]), "+v"(acc[4 + j])); }
      }
      const u32 x1 = lds0 + LDS_X1 + lane * 16, x2 = lds0 + LDS_X2 + lane * 16;
#pragma unroll
      for (int j = 0; j < 4; ++j) ST(f32x4, x1 + qw * 4096 + j * 1024) = acc[4 + j];
#pragma unroll
      for (int j = 0; j < 8; ++j) ST(f32x4, x2 + qw * 8192 + j * 1024) = acx[j];
      lds_post(lds0 + CT_Q, lane);
      lds_wait(lds0 + CT_Q, 8u);
      auto y_store = [&](f32x4 v, f32x4 w, u32 o) {
        const auto a = __builtin_amdgcn_permlane16_swap(pk_bf16(v[0], v[1]), pk_bf16(w[0], w[1]), false, false);
        const auto b = __builtin_amdgcn_permlane16_swap(pk_bf16(v[2], v[3]), pk_bf16(w[2], w[3]), false, false);
        __builtin_amdgcn_raw_buffer_store_b128((u32x4){a[0], b[0], a[1], b[1]}, wsr, WS_YT + o + (q4 & 1) * 512 + (16 * l15 + 8 * (q4 >> 1)) * 2, 0, 16);
      };
#pragma unroll
      for (int j = 0; j < 4; j += 2)
        y_store(acc[j] + LD(f32x4, x1 + (qw ^ 1) * 4096 + j * 1024), acc[j + 1] + LD(f32x4, x1 + (qw ^ 1) * 4096 + j * 1024 + 1024),
                (u32)(ch * (L * 2) + (4 * half + j) * 512));
      {
        f32x4 v[2];
#pragma unroll
        for (int j = 0; j < 2; ++j) {
          const u32 o = x2 + (2 * qw + j) * 1024;
          v[j] = (LD(f32x4, o) + LD(f32x4, o + 8192)) + (LD(f32x4, o + 16384) + LD(f32x4, o + 24576));
        }
        y_store(v[0], v[1], (u32)((((r & 1) ? D + 16 * xcd : 80 * xcd + 64) + (r >> 1)) * (L * 2) + 2 * qw * 512));
      }
      WAIT_VM(0);
      if (lds_arrive(lds0 + CT_Y, lane) == 3u) {
        if (lane == 32) put_flag(ws + WS_FLAGY + (xcd * 32 + r) * 8, tag);
        if (INY && lane < 32) put_flag(ws + WS_INY + ((lane * 8 + xcd) * 32 + r) * 8, tag);
      }
      STAMP(10);
    }
    if (qw == 0) {
      lds_wait(lds0 + CT_Y, 4u);
      lds_wait(lds0 + CT_FW, 4u);
      poll_flags<YPS>(lane < 32 ? yfl + lane * 8 : hfl + 13 * 8, tag);
      if (lane == 0) __hip_atomic_store((LAS u32*)(size_t)(lds0 + CT_FY), 1u, RLX_WG);
    } else {
      lds_wait(lds0 + CT_FY, 1u);
    }
    STAMP(11);
    const float g = __uint_as_float(__builtin_amdgcn_raw_buffer_load_b32(wsr, (16 * qw + l15) * 4, WS_GATE + (u32)(64 * m64) * 4, 16));
    u32x4 yy[3];
#pragma unroll
    for (int i = 0; i < 3; ++i) {
      const int row = (tid + 256 * i) >> 3;
      yy[i] = __builtin_amdgcn_raw_buffer_load_b128(wsr, (row < 80 ? 80 * n80 + row : D - 80 + 16 * n80 + row) * (L * 2) + (tid & 7) * 16,
                                                    WS_YT + (u32)(64 * m64 * 2), 16);
    }
    if (q4 == 0) ST(float, lds0 + LDS_OMG + (16 * qw + l15) * 4) = g;
#pragma unroll
    for (int i = 0; i < 3; ++i) ST(u32x4, lds0 + LDS_YT + ((tid + 256 * i) >> 3) * 144 + (tid & 7) * 16) = yy[i];
    if (bid == 0 && wave == 3) {
#pragma unroll 1
      for (int i = 0; i < 4; ++i) poll_flags<16>(ws + WS_FLAGY + (i * 64 + lane) * 8, tag);
      if (lane == 0) put_flag(ws, tag);
    }
  }
  STAMP(13);
  bar();
  STAMP(14);
  auto fin = [&](int unit, bool last) {
    const int tt = last ? 2 * (unit - 8) + (q4 >> 1) : unit & 3, c8 = last ? 8 + (q4 & 1) : 4 * (unit >> 2) + q4, t = 16 * tt + l15;
    const u32 yp = lds0 + LDS_YT + (8 * c8 + (l15 >> 2)) * 144 + tt * 32 + 8 * (lane & 3);
    const u32x2 ya = tr_read(yp), yb = tr_read(yp + 4 * 144);
    u32x2 za = {0, 0}, zb = {0, 0};
    if (last) { za = tr_read(yp + 16 * 144); zb = tr_read(yp + 20 * 144); }
    f32x4 s0 = LD(f32x4, lds0 + LDS_B2 + c8 * 32), s1 = LD(f32x4, lds0 + LDS_B2 + c8 * 32 + 16);
#pragma unroll
    for (int v = 0; v < 4; ++v) {
      const u32 p = lds0 + REG_B + v * SLAB + t * 336 + c8 * 32;
      s0 += LD(f32x4, p); s1 += LD(f32x4, p + 16);
    }
    const u32x4 ud = LD(u32x4, lds0 + LDS_GA + t * 160 + c8 * 16);
    const float g = LD(float, lds0 + LDS_OMG + t * 4), om = rbf(1.f - g);
    u32x4 o;
#pragma unroll
    for (int e = 0; e < 4; ++e) {
      const u32 y = e < 2 ? ya[e] : yb[e - 2], y2 = e < 2 ? za[e] : zb[e - 2];
      float a0 = bflo(y) + bflo(ud[e]), a1 = bfhi(y) + bfhi(ud[e]);
      if (last) { a0 += bflo(y2); a1 += bfhi(y2); }
      const float x0 = e < 2 ? s0[2 * e] : s1[2 * e - 4], x1 = e < 2 ? s0[2 * e + 1] : s1[2 * e - 3];
      o[e] = pk_bf16(g * a0 + om * x0, g * a1 + om * x1);
    }
    *(u32x4*)(P.out + (size_t)(64 * m64 + t) * D + 80 * n80 + 8 * c8) = o;
  };
  if (wave < 2) { fin(wave, false); fin(wave + 8, true); } else fin(wave, false);
  STAMP(15);
}

extern "C" void kernel_launch(void* const* d_in, const int* in_sizes, int n_in, void* d_out, int out_size,
                              void* d_ws, size_t ws_size, hipStream_t stream) {
  Params P;
  P.u = (const u16*)d_in[0]; P.lar = (const u16*)d_in[1]; P.aim = (const u16*)d_in[2]; P.cre = (const u16*)d_in[3];
  P.cim = (const u16*)d_in[4]; P.ldt = (const u16*)d_in[5]; P.dsk = (const u16*)d_in[6]; P.w1 = (const u16*)d_in[8];
  P.b1 = (const u16*)d_in[9]; P.w2 = (const u16*)d_in[10]; P.b2 = (const u16*)d_in[11]; P.gw = (const u16*)d_in[12];
  P.out = (u16*)d_out; P.ws = (char*)d_ws;
  (void)hipFuncSetAttribute((const void*)fused, hipFuncAttributeMaxDynamicSharedMemorySize, LDS_TOTAL);
  fused<<<256, 512, LDS_TOTAL, stream>>>(P);
}
```

```cpp
#include <hip/hip_runtime.h>
#include <hip/hip_bf16.h>

typedef unsigned short u16;
typedef unsigned int u32;
typedef unsigned long long u64;
typedef short s16x4 __attribute__((ext_vector_type(4)));
typedef __bf16 bf16x2 __attribute__((ext_vector_type(2)));
typedef __bf16 bf16x8 __attribute__((ext_vector_type(8)));
typedef _Float16 f16x2 __attribute__((ext_vector_type(2)));
typedef _Float16 f16x8 __attribute__((ext_vector_type(8)));
typedef float f32x2 __attribute__((ext_vector_type(2)));
typedef float f32x4 __attribute__((ext_vector_type(4)));
typedef u32 u32x2 __attribute__((ext_vector_type(2)));
typedef int i32x4 __attribute__((ext_vector_type(4)));
typedef u32 u32x4 __attribute__((ext_vector_type(4)));
typedef __amdgpu_buffer_rsrc_t rsrc_t;
#define LAS __attribute__((address_space(3)))
#define GAS __attribute__((address_space(1)))
#define RLX_AGENT __ATOMIC_RELAXED, __HIP_MEMORY_SCOPE_AGENT
#define RLX_WG __ATOMIC_RELAXED, __HIP_MEMORY_SCOPE_WORKGROUP
#define DEV __device__ __forceinline__
#define HSLEEP 40
#define G2PRIO 1
#define CVPRIO 2
#define KTPRIO 0
#define G2DP 3
#define YGATE 8
#define YPS 4
#define HWAVE 0
#define INH 1
#define INY 0
#define HPS 4
#define SCONS 1
#define STLATE 1
#define STAG 0
#define SB() __builtin_amdgcn_sched_barrier(0)

constexpr int L = 2048, D = 640, NM = 128, F = 1728;
constexpr u32 WS_FLAGK = 2048;
constexpr u32 WS_FLAGW = 12288;
constexpr u32 WS_FLAGS = 4096;
constexpr u32 WS_FLAGH = WS_FLAGS + 2048;
constexpr u32 WS_FLAGY = WS_FLAGH + 2048;
constexpr u32 WS_INH = 16384;
constexpr u32 WS_INY = 49152;
constexpr u32 WS_GATE = 114688;
constexpr u32 WS_KX = 131072;
constexpr u32 WS_UT = WS_KX + 128 * 4096;
constexpr u32 WS_YT = WS_UT + D * L * 2;
constexpr u32 WS_HT = WS_YT + (D + 128) * L * 2;
constexpr u32 WS_W2T = WS_HT + 32 * 54 * 4096;
constexpr u32 WS_END = WS_W2T + 8 * 54 * 5120;
#define STAMP(k)
constexpr u32 SCR_W = 15616;
constexpr u32 REG_A = 0, REG_B = 65536;
constexpr u32 SLAB = 64 * 336;
constexpr u32 LDS_SIDE = 131072;
constexpr u32 LDS_X1 = REG_B + 24576, LDS_X2 = REG_B + 40960;
constexpr u32 LDS_GW = LDS_SIDE + 20480;
constexpr u32 LDS_B1 = LDS_GW + 2048;
constexpr u32 LDS_PAR = LDS_B1 + 1024;
constexpr u32 LDS_GA = LDS_GW;
constexpr u32 LDS_OMG = LDS_GA + 10240;
constexpr u32 LDS_B2 = LDS_OMG + 256;
constexpr u32 LDS_CTL = LDS_B2 + 320;
constexpr u32 LDS_TOTAL = LDS_CTL + 64;
#define DBG(i)
constexpr u32 CH_KC0 = 0, CH_K = 2576, CH_U = CH_K + 4608, UCP = 4160, CH_KC1 = 2 * SCR_W - 2576;
constexpr u32 LDS_YT = 0;
constexpr u32 CT_PAIR = LDS_CTL, CT_H = LDS_CTL + 16, CT_Y = LDS_CTL + 20, CT_R = LDS_CTL + 24, CT_FH = LDS_CTL + 28, CT_FY = LDS_CTL + 32, CT_FS = LDS_CTL + 36, CT_Q = LDS_CTL + 40, CT_A = LDS_CTL + 48, CT_S = LDS_CTL + 52, CT_FW = LDS_CTL + 56;

struct Params {
  const u16 *u, *lar, *aim, *cre, *cim, *ldt, *dsk, *w1, *b1, *w2, *b2, *gw;
  u16* out;
  char* ws;
};

DEV float bf2f(u32 x) { return __uint_as_float(x << 16); }
DEV float bflo(u32 x) { return __uint_as_float(x << 16); }
DEV float bfhi(u32 x) { return __uint_as_float(x & 0xffff0000u); }
DEV u32 pk_bf16(float a, float b) { f32x2 v = {a, b}; bf16x2 r = __builtin_convertvector(v, bf16x2); return __builtin_bit_cast(u32, r); }
DEV float rbf(float a) { return bflo(pk_bf16(a, a)); }
DEV u32 pk_f16(f32x2 v) { f16x2 r = __builtin_convertvector(v, f16x2); return __builtin_bit_cast(u32, r); }
DEV f32x2 cmul(f32x2 a, f32x2 b) {
  f32x2 t, r;
  asm("v_pk_mul_f32 %0, %1, %2 op_sel:[0,0] op_sel_hi:[0,1]" : "=v"(t) : "v"(a), "v"(b));
  asm("v_pk_fma_f32 %0, %1, %2, %3 op_sel:[1,1,0] op_sel_hi:[1,0,1] neg_lo:[1,0,0]" : "=v"(r) : "v"(a), "v"(b), "v"(t));
  return r;
}
DEV f32x2 cexp_p(float ar, float ai, float p) {
  float e = __builtin_amdgcn_exp2f(ar * p * 1.44269504088896341f);
  float t = ai * p;
  float hi = t * 0.15915494309189535f;
  float lo = __builtin_fmaf(t, 0.15915494309189535f, -hi);
  lo = __builtin_fmaf(t, 6.4206383e-9f, lo);
  float fr = (hi - __builtin_rintf(hi)) + lo;
  return (f32x2){e * __builtin_amdgcn_cosf(fr), e * __builtin_amdgcn_sinf(fr)};
}
#define WAIT_VM(n) asm volatile("s_waitcnt vmcnt(" #n ")" ::: "memory")
#define WAIT_LGKM0() asm volatile("s_waitcnt lgkmcnt(0)" ::: "memory")
DEV void bar() { asm volatile("s_waitcnt lgkmcnt(0)" ::: "memory"); __builtin_amdgcn_s_barrier(); asm volatile("" ::: "memory"); }
DEV void rawbar() { asm volatile("" ::: "memory"); __builtin_amdgcn_s_barrier(); asm volatile("" ::: "memory"); }

DEV void glds1(const void* base, u32 voff, u32 lds) {
  u32 keep;
  asm volatile("s_mov_b32 %0, m0\n\ts_mov_b32 m0, %3\n\ts_nop 0\n\tglobal_load_lds_dwordx4 %1, %2\n\ts_mov_b32 m0, %0"
               : "=&s"(keep) : "v"(voff), "s"(base), "s"(lds) : "memory");
}
DEV void glds1p(const void* p, u32 lds) {
  u32 keep;
  asm volatile("s_mov_b32 %0, m0\n\ts_mov_b32 m0, %2\n\ts_nop 0\n\tglobal_load_lds_dwordx4 %1, off\n\ts_mov_b32 m0, %0"
               : "=&s"(keep) : "v"(p), "s"(lds) : "memory");
}
DEV void glds4(rsrc_t rs, u32 v0, u32 v1, u32 v2, u32 v3, u32 s, u32 step, u32 lds) {
  u32 keep;
  asm volatile("s_mov_b32 %0, m0\n\ts_mov_b32 m0, %10\n\ts_nop 0\n\t"
               "buffer_load_dwordx4 %1, %5, %6 offen lds\n\t"
               "buffer_load_dwordx4 %2, %5, %7 offen offset:1024 lds\n\t"
               "buffer_load_dwordx4 %3, %5, %8 offen offset:2048 lds\n\t"
               "buffer_load_dwordx4 %4, %5, %9 offen offset:3072 lds\n\t"
               "s_mov_b32 m0, %0"
               : "=&s"(keep) : "v"(v0), "v"(v1), "v"(v2), "v"(v3), "s"(rs), "s"(s), "s"(s + step),
                 "s"(s + 2 * step), "s"(s + 3 * step), "s"(lds) : "memory");
}
DEV u32x2 tr_read(u32 a) {
  s16x4 r = __builtin_amdgcn_ds_read_tr16_b64_v4i16((LAS s16x4*)(size_t)a);
  return __builtin_bit_cast(u32x2, r);
}
DEV bf16x8 mk8(u32x2 a, u32x2 b) { u32x4 v = {a.x, a.y, b.x, b.y}; return __builtin_bit_cast(bf16x8, v); }
DEV f32x4 mma(bf16x8 a, bf16x8 b, f32x4 c) { return __builtin_amdgcn_mfma_f32_16x16x32_bf16(a, b, c, 0, 0, 0); }
#define LD(T, a) (*(const LAS T*)(size_t)(a))
#define ST(T, a) (*(LAS T*)(size_t)(a))

template <int SLEEP>
DEV void poll_flags(const char* p, u64 tag) {
#pragma unroll 1
  for (u32 spins = 0; spins < (1u << 18); ++spins) {
    u64 v = __hip_atomic_load((const GAS u64*)p, RLX_AGENT);
    if (__all(v == tag)) break;
    __builtin_amdgcn_s_sleep(SLEEP);
  }
  __builtin_amdgcn_fence(__ATOMIC_ACQUIRE, "wavefront");
  asm volatile("" ::: "memory");
}
DEV void put_flag(char* p, u64 tag) { __hip_atomic_store((GAS u64*)p, tag, RLX_AGENT); }
DEV u32 lds_arrive(u32 a, int lane) {
  WAIT_LGKM0();
  u32 r = 0;
  if (lane == 0) r = __hip_atomic_fetch_add((LAS u32*)(size_t)a, 1u, RLX_WG);
  return __builtin_amdgcn_readfirstlane(r);
}
DEV void lds_post(u32 a, int lane) {
  asm volatile("" ::: "memory");
  if (lane == 0) __hip_atomic_fetch_add((LAS u32*)(size_t)a, 1u, RLX_WG);
}
DEV void lds_wait(u32 a, u32 target) {
#pragma unroll 1
  for (u32 spins = 0; spins < (1u << 22); ++spins) {
    u32 v = __builtin_amdgcn_readfirstlane(__hip_atomic_load((LAS u32*)(size_t)a, RLX_WG));
    if (v >= target) break;
    __builtin_amdgcn_s_sleep(1);
  }
  asm volatile("" ::: "memory");
}

DEV void kgen(u32 pp, float ldt, u32 (&pk)[57], float& kscale) {
  const float dt = rbf(expf(ldt));
  const float ar = -rbf(expf(bf2f(LD(u16, pp)))) * dt, ai = bf2f(LD(u16, pp + 128)) * dt;
  const float cr = 2.f * bf2f(LD(u16, pp + 256)) * dt, ci = 2.f * bf2f(LD(u16, pp + 384)) * dt;
  u32 mx = max(__float_as_uint(cr) & 0x7fffffffu, __float_as_uint(ci) & 0x7fffffffu);
  mx = max(mx, (u32)__builtin_amdgcn_update_dpp(0, mx, 0x128, 0xf, 0xf, false));
  mx = max(mx, (u32)__builtin_amdgcn_update_dpp(0, mx, 0x124, 0xf, 0xf, false));
  mx = max(mx, (u32)__builtin_amdgcn_update_dpp(0, mx, 0x122, 0xf, 0xf, false));
  mx = max(mx, (u32)__builtin_amdgcn_update_dpp(0, mx, 0x121, 0xf, 0xf, false));
  u32 eb = max(max((u32)__builtin_amdgcn_readlane(mx, 0), (u32)__builtin_amdgcn_readlane(mx, 16)),
               max((u32)__builtin_amdgcn_readlane(mx, 32), (u32)__builtin_amdgcn_readlane(mx, 48))) >> 23;
  eb = eb < 40u ? 40u : (eb > 230u ? 230u : eb);
  const float inv = __uint_as_float((127u + 127u + 12u - eb) << 23);
  kscale = __uint_as_float((eb - 12u - 14u) << 23);
  const f32x2 e1 = cexp_p(ar, ai, 1.f), e8 = cexp_p(ar, -ai, 8.f), e128 = cexp_p(ar, ai, 128.f);
  {
    f32x2 G[16];
    const f32x2 e16 = cmul(e8, e8), e32 = cmul(e16, e16), e64 = cmul(e32, e32);
    G[0] = (f32x2){cr * inv, -ci * inv};
    G[1] = cmul(G[0], e8);
#pragma unroll
    for (int j = 0; j < 2; ++j) G[2 + j] = cmul(G[j], e16);
#pragma unroll
    for (int j = 0; j < 4; ++j) G[4 + j] = cmul(G[j], e32);
#pragma unroll
    for (int j = 0; j < 8; ++j) G[8 + j] = cmul(G[j], e64);
#pragma unroll
    for (int j = 0; j < 16; ++j) pk[j] = pk_f16(G[j]);
  }
  f32x2 Pw[8], Q[17];
  const f32x2 e2 = cmul(e1, e1), e4 = cmul(e2, e2);
  Pw[1] = e1; Pw[2] = e2; Pw[3] = cmul(e1, e2); Pw[4] = e4; Pw[5] = cmul(e1, e4); Pw[6] = cmul(e2, e4); Pw[7] = cmul(Pw[3], e4);
  const f32x2 e256 = cmul(e128, e128), e512 = cmul(e256, e256), e1024 = cmul(e512, e512);
  Q[1] = e128 * 16384.f; Q[2] = e256 * 16384.f; Q[4] = e512 * 16384.f; Q[8] = e1024 * 16384.f;
  Q[3] = cmul(Q[1], e256);
  Q[5] = cmul(Q[1], e512); Q[6] = cmul(Q[2], e512); Q[7] = cmul(Q[3], e512);
#pragma unroll
  for (int j = 1; j < 9; ++j) Q[8 + j] = cmul(Q[j], e1024);
  pk[16] = 0x00007400u;
#pragma unroll
  for (int j = 1; j < 8; ++j) pk[16 + j] = pk_f16(Pw[j] * 16384.f);
  pk[24] = pk_f16(Q[1]);
#pragma unroll
  for (int j = 1; j < 8; ++j) pk[24 + j] = pk_f16(cmul(Q[1], Pw[j]));
#pragma unroll
  for (int i = 2; i < 4; ++i) {
    pk[32 + 4 * (i - 2)] = pk_f16(Q[i]);
#pragma unroll
    for (int j = 1; j < 4; ++j) pk[32 + 4 * (i - 2) + j] = pk_f16(cmul(Q[i], Pw[2 * j]));
  }
#pragma unroll
  for (int i = 4; i < 8; ++i) { pk[40 + 2 * (i - 4)] = pk_f16(Q[i]); pk[41 + 2 * (i - 4)] = pk_f16(cmul(Q[i], Pw[4])); }
#pragma unroll
  for (int i = 8; i < 17; ++i) pk[48 + i - 8] = pk_f16(Q[i]);
}
template <int OFF>
DEV void put_rows(u32 base, const u32 (&pk)[57]) {
  u32 keep;
  asm volatile("s_mov_b32 %0, m0\n\ts_mov_b32 m0, %20\n\ts_nop 0\n\t"
               "ds_write_addtid_b32 %1 offset:%c21+0\n\t"
               "ds_write_addtid_b32 %2 offset:%c21+272\n\t"
               "ds_write_addtid_b32 %3 offset:%c21+544\n\t"
               "ds_write_addtid_b32 %4 offset:%c21+816\n\t"
               "ds_write_addtid_b32 %5 offset:%c21+1088\n\t"
               "ds_write_addtid_b32 %6 offset:%c21+1360\n\t"
               "ds_write_addtid_b32 %7 offset:%c21+1632\n\t"
               "ds_write_addtid_b32 %8 offset:%c21+1904\n\t"
               "ds_write_addtid_b32 %9 offset:%c21+2176\n\t"
               "ds_write_addtid_b32 %10 offset:%c21+2448\n\t"
               "ds_write_addtid_b32 %11 offset:%c21+2720\n\t"
               "ds_write_addtid_b32 %12 offset:%c21+2992\n\t"
               "ds_write_addtid_b32 %13 offset:%c21+3264\n\t"
               "ds_write_addtid_b32 %14 offset:%c21+3536\n\t"
               "ds_write_addtid_b32 %15 offset:%c21+3808\n\t"
               "ds_write_addtid_b32 %16 offset:%c21+4080\n\t"
               "ds_write_addtid_b32 %17 offset:%c21+4352\n\t"
               "ds_write_addtid_b32 %18 offset:%c21+4624\n\t"
               "ds_write_addtid_b32 %19 offset:%c21+4896\n\t"
               "s_mov_b32 m0, %0"
               : "=&s"(keep) : "v"(pk[0]), "v"(pk[1]), "v"(pk[2]), "v"(pk[3]), "v"(pk[4]), "v"(pk[5]), "v"(pk[6]), "v"(pk[7]), "v"(pk[8]), "v"(pk[9]), "v"(pk[10]), "v"(pk[11]), "v"(pk[12]), "v"(pk[13]), "v"(pk[14]), "v"(pk[15]), "v"(pk[16]), "v"(pk[17]), "v"(pk[18]), "s"(base), "i"(OFF) : "memory");
  asm volatile("s_mov_b32 %0, m0\n\ts_mov_b32 m0, %20\n\ts_nop 0\n\t"
               "ds_write_addtid_b32 %1 offset:%c21+5168\n\t"
               "ds_write_addtid_b32 %2 offset:%c21+5440\n\t"
               "ds_write_addtid_b32 %3 offset:%c21+5712\n\t"
               "ds_write_addtid_b32 %4 offset:%c21+5984\n\t"
               "ds_write_addtid_b32 %5 offset:%c21+6256\n\t"
               "ds_write_addtid_b32 %6 offset:%c21+6528\n\t"
               "ds_write_addtid_b32 %7 offset:%c21+6800\n\t"
               "ds_write_addtid_b32 %8 offset:%c21+7072\n\t"
               "ds_write_addtid_b32 %9 offset:%c21+7344\n\t"
               "ds_write_addtid_b32 %10 offset:%c21+7616\n\t"
               "ds_write_addtid_b32 %11 offset:%c21+7888\n\t"
               "ds_write_addtid_b32 %12 offset:%c21+8160\n\t"
               "ds_write_addtid_b32 %13 offset:%c21+8432\n\t"
               "ds_write_addtid_b32 %14 offset:%c21+8704\n\t"
               "ds_write_addtid_b32 %15 offset:%c21+8976\n\t"
               "ds_write_addtid_b32 %16 offset:%c21+9248\n\t"
               "ds_write_addtid_b32 %17 offset:%c21+9520\n\t"
               "ds_write_addtid_b32 %18 offset:%c21+9792\n\t"
               "ds_write_addtid_b32 %19 offset:%c21+10064\n\t"
               "s_mov_b32 m0, %0"
               : "=&s"(keep) : "v"(pk[19]), "v"(pk[20]), "v"(pk[21]), "v"(pk[22]), "v"(pk[23]), "v"(pk[24]), "v"(pk[25]), "v"(pk[26]), "v"(pk[27]), "v"(pk[28]), "v"(pk[29]), "v"(pk[30]), "v"(pk[31]), "v"(pk[32]), "v"(pk[33]), "v"(pk[34]), "v"(pk[35]), "v"(pk[36]), "v"(pk[37]), "s"(base), "i"(OFF) : "memory");
  asm volatile("s_mov_b32 %0, m0\n\ts_mov_b32 m0, %20\n\ts_nop 0\n\t"
               "ds_write_addtid_b32 %1 offset:%c21+10336\n\t"
               "ds_write_addtid_b32 %2 offset:%c21+10608\n\t"
               "ds_write_addtid_b32 %3 offset:%c21+10880\n\t"
               "ds_write_addtid_b32 %4 offset:%c21+11152\n\t"
               "ds_write_addtid_b32 %5 offset:%c21+11424\n\t"
               "ds_write_addtid_b32 %6 offset:%c21+11696\n\t"
               "ds_write_addtid_b32 %7 offset:%c21+11968\n\t"
               "ds_write_addtid_b32 %8 offset:%c21+12240\n\t"
               "ds_write_addtid_b32 %9 offset:%c21+12512\n\t"
               "ds_write_addtid_b32 %10 offset:%c21+12784\n\t"
               "ds_write_addtid_b32 %11 offset:%c21+13056\n\t"
               "ds_write_addtid_b32 %12 offset:%c21+13328\n\t"
               "ds_write_addtid_b32 %13 offset:%c21+13600\n\t"
               "ds_write_addtid_b32 %14 offset:%c21+13872\n\t"
               "ds_write_addtid_b32 %15 offset:%c21+14144\n\t"
               "ds_write_addtid_b32 %16 offset:%c21+14416\n\t"
               "ds_write_addtid_b32 %17 offset:%c21+14688\n\t"
               "ds_write_addtid_b32 %18 offset:%c21+14960\n\t"
               "ds_write_addtid_b32 %19 offset:%c21+15232\n\t"
               "s_mov_b32 m0, %0"
               : "=&s"(keep) : "v"(pk[38]), "v"(pk[39]), "v"(pk[40]), "v"(pk[41]), "v"(pk[42]), "v"(pk[43]), "v"(pk[44]), "v"(pk[45]), "v"(pk[46]), "v"(pk[47]), "v"(pk[48]), "v"(pk[49]), "v"(pk[50]), "v"(pk[51]), "v"(pk[52]), "v"(pk[53]), "v"(pk[54]), "v"(pk[55]), "v"(pk[56]), "s"(base), "i"(OFF) : "memory");
}
template <bool ANY>
DEV void ktable(u32 scr, u32 kb, const u32 (&pk)[57], float kscale, int lane) {
  const int l15 = lane & 15, q4 = lane >> 4;
  if (!ANY || scr < 65536) put_rows<0>(scr, pk); else put_rows<49152>(scr - 49152, pk);
  asm volatile("" ::: "memory");
  {
    f32x4 kc[3];
#pragma unroll
    for (int T = 0; T < 3; ++T) kc[T] = (f32x4){0.f, 0.f, 0.f, 0.f};
    const u32 rp = scr + l15 * 272 + q4 * 16;
    const u32 rp2 = scr + (48 + (l15 > 8 ? 8 : l15)) * 272 + q4 * 16;
    f16x8 fr[4][4];
#pragma unroll
    for (int ks = 0; ks < 4; ++ks) {
      fr[ks][0] = LD(f16x8, rp + ks * 64); fr[ks][1] = LD(f16x8, rp + 16 * 272 + ks * 64);
      fr[ks][2] = LD(f16x8, rp + 32 * 272 + ks * 64); fr[ks][3] = LD(f16x8, rp2 + ks * 64);
    }
    SB();
#pragma unroll
    for (int ks = 0; ks < 4; ++ks)
#pragma unroll
      for (int T = 0; T < 3; ++T) kc[T] = __builtin_amdgcn_mfma_f32_16x16x32_f16(fr[ks][1 + T], fr[ks][0], kc[T], 0, 0, 0);
    WAIT_LGKM0();
    ST(f32x4, kb + (128 * (q4 >> 1) + 8 * l15 + 4 * (q4 & 1)) * 4) = kc[0] * kscale;
    const f32x4 v1 = kc[1] * kscale, v2 = kc[2] * kscale;
    if (q4 < 2) {
      ST(f32x4, kb + (256 + 64 * q4 + 4 * l15) * 4) = v1;
#pragma unroll
      for (int rr = 0; rr < 4; ++rr) ST(float, kb + (512 + 16 * (4 * q4 + rr) + l15) * 4) = v2[rr];
    } else {
      ST(f32x2, kb + (384 + 64 * (q4 - 2) + 2 * l15) * 4) = (f32x2){v1[0], v1[1]};
      ST(f32x2, kb + (416 + 64 * (q4 - 2) + 2 * l15) * 4) = (f32x2){v1[2], v1[3]};
      if (lane == 32) ST(float, kb + 640 * 4) = v2[0];
    }
  }
}
DEV void expand(u32 chb, int half, int lane, u32x4& c0, u32x4& c1) {
  {
    auto tab = [&](int i) { return LD(float, chb + CH_KC0 + i * 4) + LD(float, chb + CH_KC1 + i * 4); };
    const int m = 255 - 64 * half - lane;
    const float a = tab(512 + m - 127), b = tab(512 + m - 128), c = (m & 1) ? a : b;
    const u32 bb = pk_bf16(b, b);
    c0 = (u32x4){pk_bf16(a, a), pk_bf16(a, c), bb, bb};
    if (half == 0) {
      const int i = 384 + 2 * (63 - lane);
      const float x = tab(i + 2), y = tab(i + 1), z = tab(i);
      c1 = (u32x4){pk_bf16(x, x), pk_bf16(y, y), pk_bf16(y, z), pk_bf16(z, z)};
    } else {
      const bool lo = lane >= 32;
      const int i = lo ? 8 * (63 - lane) : 256 + 4 * (31 - lane);
      const f32x4 s0 = LD(f32x4, chb + CH_KC0 + i * 4) + LD(f32x4, chb + CH_KC1 + i * 4);
      const f32x4 s1 = LD(f32x4, chb + CH_KC0 + i * 4 + 16) + LD(f32x4, chb + CH_KC1 + i * 4 + 16);
      c1 = (u32x4){pk_bf16(lo ? s1[3] : s1[0], lo ? s1[2] : s0[3]), pk_bf16(lo ? s1[1] : s0[2], lo ? s1[0] : s0[2]),
                   pk_bf16(lo ? s0[3] : s0[2], lo ? s0[2] : s0[1]), pk_bf16(lo ? s0[1] : s0[0], s0[0])};
    }
  }
}

typedef u32x2 __attribute__((aligned(4))) u32x2a;
DEV u32x2 ld2(u32 a) { return *(const LAS u32x2a*)(size_t)a; }
struct ConvFrags { bf16x8 A[2][8], B[2][8]; };
template <bool NEXT>
DEV void conv_step(ConvFrags& f, int set, u32 pa, u32 pa8, u32 pb, f32x4 (&acc)[8]) {
  u32x2 lo[8], hi[8];
#pragma unroll
  for (int g = 0; g < 12; ++g) {
    if (NEXT) {
#pragma unroll
      for (int d = 2 * g; d < 2 * g + 2; ++d) {
        const int k = d / 3;
        if (d % 3 == 0) lo[k] = ld2(pa + 512 * k);
        if (d % 3 == 1) { hi[k] = ld2(pa8 + 512 * k); f.A[set ^ 1][k] = mk8(lo[k], hi[k]); }
        if (d % 3 == 2) f.B[set ^ 1][7 - k] = LD(bf16x8, pb + 512 * k);
      }
    }
#pragma unroll
    for (int j = 3 * g; j < 3 * g + 3; ++j) {
      const int k = j < 1 ? 0 : j < 3 ? 1 : j < 6 ? 2 : j < 10 ? 3 : j < 15 ? 4 : j < 21 ? 5 : j < 28 ? 6 : 7, e = j - k * (k + 1) / 2;
      acc[e + 7 - k] = mma(f.A[set][e], f.B[set][7 - k], acc[e + 7 - k]);
    }
    if (NEXT) {
#pragma unroll
      for (int j = 0; j < 2; ++j) { __builtin_amdgcn_sched_group_barrier(0x8, 1, 0); __builtin_amdgcn_sched_group_barrier(0x100, 1, 0); }
      __builtin_amdgcn_sched_group_barrier(0x8, 1, 0);
    }
    SB();
  }
}

__global__ __launch_bounds__(512, 2) void fused(Params P) {
  extern __shared__ __attribute__((aligned(16))) char smem[];
  const int tid = threadIdx.x, lane = tid & 63;
  const int wave = __builtin_amdgcn_readfirstlane(tid >> 6);
  const int bid = blockIdx.x, xcd = bid & 7, r = bid >> 3;
  const bool regular = r < 28;
  const int quad = (wave >> 2) ^ 1, qw = wave & 3, half = qw & 1;
  const int l15 = lane & 15, q4 = lane >> 4;
  const u32 lds0 = (u32)(size_t)(LAS char*)smem;
  char* ws = P.ws;
  const int rq = (r * 37) >> 8, nl = r - 7 * rq, mi = (xcd >> 1) * 4 + rq, ni = (xcd & 1) * 7 + nl;
  const int rot = nl + 7 * rq - 10 * ((nl + 7 * rq) / 10);
  const int m64 = (xcd >> 1) * 8 + (r >> 2), n80 = (xcd & 1) * 4 + (r & 3);
  const rsrc_t wsr = __builtin_amdgcn_make_buffer_rsrc(ws, 0, WS_END, 0x00020000);
  const int chan0 = 80 * xcd + 2 * r;
  const u32 chb = lds0 + REG_A + (qw >> 1) * (2 * SCR_W);
  const int ch = chan0 + (qw >> 1);
  u64 tag;
  const char* hfl = INH ? ws + WS_INH + (m64 * 8 + n80) * 128 : ws + WS_FLAGH + (m64 >> 1) * 128;
  const char* yfl = INY ? ws + WS_INY + (m64 * 8 + n80) * 256 : ws + WS_FLAGY + n80 * 256;

  auto par_load = [&](int ch, u32 dst) {
    const int a = (lane >> 3) & 3;
    const u16 *p0 = P.lar, *p1 = P.aim, *p2 = P.cre, *p3 = P.cim;
    asm volatile("" : "+s"(p0), "+s"(p1), "+s"(p2), "+s"(p3));
    const u16* p = a == 0 ? p0 : a == 1 ? p1 : a == 2 ? p2 : p3;
    if (lane < 32) glds1p(p + ch * NM + 64 * half + (lane & 7) * 8, dst);
  };
  const bool isw = qw >= 2, side = quad ? !isw : isw && m64 < 27;
  auto side_load = [&]() {
#pragma unroll
    for (int i = 0; i < 5; ++i) {
      const int o = i * 64 + lane, row = (o * 6554) >> 16, c = o - 10 * row;
      glds1(isw ? P.w2 : P.u, (u32)((m64 * 64 + half * 32 + row) * (D * 2) + n80 * 160 + c * 16), lds0 + LDS_SIDE + qw * 5120 + i * 1024);
    }
  };
  u32x4 sv[5];
  auto side_read = [&]() {
    const u32 p = lds0 + LDS_SIDE + qw * 5120 + ((isw ? 4 : 8) * q4 + (l15 >> 2)) * 160 + 8 * (lane & 3), p2 = p + (isw ? 16 * 160 : 4 * 160);
#pragma unroll
    for (int i = 0; i < 5; ++i) { const u32x2 a = tr_read(p + 32 * i), b = tr_read(p2 + 32 * i); sv[i] = (u32x4){a.x, a.y, b.x, b.y}; }
  };
  auto side_store = [&]() {
    const u32 o = isw ? WS_W2T + (u32)((n80 * 54 + 2 * m64 + half) * 5120) + lane * 16
                      : WS_UT + (u32)((80 * n80 + l15) * (L * 2) + (64 * m64 + 32 * half) * 2 + q4 * 16);
#pragma unroll
    for (int i = 0; i < 5; ++i) __builtin_amdgcn_raw_buffer_store_b128(sv[i], wsr, o + i * (isw ? 1024 : 16 * L * 2), 0, 16);
  };

  u32 ldt2 = 0;
  if (!regular) {
    const int xi = 4 * (r - 28) + 2 * quad + (qw >> 1);
    par_load(80 * xcd + 64 + xi, lds0 + LDS_PAR + (4 + wave) * 512);
    if (quad == 0) par_load(chan0 + (qw >> 1), lds0 + LDS_PAR + qw * 512);
    if (side) side_load();
    if (tid < 16) ST(u32, lds0 + LDS_CTL + tid * 4) = 0;
    u32 ldx;
    asm volatile("s_load_dwordx2 %0, %3, 0x0\n\ts_load_dword %1, %4, 0x0\n\ts_load_dword %2, %5, 0x0\n\ts_waitcnt lgkmcnt(0)"
                 : "=&s"(tag), "=&s"(ldt2), "=&s"(ldx) : "s"(ws), "s"(P.ldt + chan0), "s"(P.ldt + 80 * xcd + 64 + (xi & ~1)) : "memory");
    tag += 1;
    if (side) WAIT_VM(5); else WAIT_VM(0);
    u32 pk[57];
    float kscale;
    kgen(lds0 + LDS_PAR + (4 + wave) * 512 + lane * 2, (xi & 1) ? bfhi(ldx) : bflo(ldx), pk, kscale);
#pragma unroll
    for (int j = 0; j < 57; ++j) asm volatile("" : "+v"(pk[j]));
    WAIT_VM(0);
    STAMP(1);
    if (side) { side_read(); side_store(); }
    const u32 xb = chb + (quad ? REG_B : 0);
    ktable<true>(xb + half * SCR_W, xb + (half ? CH_KC1 : CH_KC0), pk, kscale, lane);
    bar();
    u32x4 c0, c1;
    expand(xb, half, lane, c0, c1);
    const u32 ko = WS_KX + (u32)((xcd * 16 + xi) * 4096 + half * 1024) + lane * 16;
    __builtin_amdgcn_raw_buffer_store_b128(c0, wsr, ko, 0, 16);
    __builtin_amdgcn_raw_buffer_store_b128(c1, wsr, ko + 2048, 0, 16);
    WAIT_VM(0);
    bar();
    if (tid == 0) {
      put_flag(ws + WS_FLAGK + (xcd * 4 + r - 28) * 8, tag); put_flag(ws + WS_FLAGS + (n80 * 32 + m64) * 8, tag);
      put_flag(ws + WS_FLAGW + (n80 * 32 + m64) * 8, tag);
    }
    STAMP(2);
    if (quad == 1) {
      u32 acc = 0;
#pragma unroll
      for (int i = 0; i < 3; ++i) {
        const int ln = (r - 28) * 640 + i * 256 + tid, row = (ln * 13108) >> 16, rr = 512 * (xcd >> 1) + row;
        if (i * 256 + tid < 640 && rr < F) acc ^= *(const u32*)((const char*)P.w2 + rr * (D * 2) + (xcd & 1) * 640 + (ln - 5 * row) * 128);
      }
      asm volatile("" :: "v"(acc));
      lds_post(lds0 + CT_R, lane);
      STAMP(3);
    }
  }

  if (quad == 0) {
    const rsrc_t xr = __builtin_amdgcn_make_buffer_rsrc((void*)P.u, 0, L * D * 2, 0x00020000);
    const rsrc_t wr = __builtin_amdgcn_make_buffer_rsrc((void*)P.w1, 0, D * F * 2, 0x00020000);
    const u32 xv = (u32)((lane >> 3) * (D * 2) + ((lane & 7) ^ q4) * 16);
    const u32 wv = (u32)(q4 * (F * 2) + (l15 ^ (q4 << 2)) * 16);
    const u32 xs = (u32)((mi * 128 + 32 * qw) * (D * 2)), wso = (u32)(16 * qw * (F * 2) + ni * 256);
    auto stage = [&](int i) {
      const u32 dst = lds0 + ((i + 2) & 3) * 32768 + qw * 4096;
      const int k = i + rot >= 10 ? i + rot - 10 : i + rot;
      glds4(xr, xv, xv ^ 64, xv, xv ^ 64, xs + k * 128, 8 * D * 2 - 1024, dst);
      glds4(wr, wv, wv ^ 16, wv ^ 32, wv ^ 48, wso + k * (64 * F * 2), 4 * F * 2 - 1024, dst + 16384);
    };
    if (regular) {
      __builtin_amdgcn_s_setprio(3);
      rawbar();
      par_load(chan0 + (qw >> 1), lds0 + LDS_PAR + qw * 512);
      stage(0);
      if (qw == 0) glds1(P.b1, (u32)(ni * 256 + (lane & (ni == 13 ? 7 : 15)) * 16), lds0 + LDS_B1);
      if (qw == 1 && ni == 13) { glds1(P.gw, lane * 16, lds0 + LDS_GW); glds1(P.gw, 1024 + l15 * 16, lds0 + LDS_GW + 1024); }
      stage(1);
      asm volatile("s_load_dwordx2 %0, %2, 0x0\n\ts_load_dword %1, %3, 0x0\n\ts_waitcnt lgkmcnt(0)"
                   : "=&s"(tag), "=&s"(ldt2) : "s"(ws), "s"(P.ldt + chan0) : "memory");
      tag += 1;
      WAIT_VM(16);
    }
    u32 pk[57];
    float kscale;
    kgen(lds0 + LDS_PAR + qw * 512 + lane * 2, (qw >> 1) ? bfhi(ldt2) : bflo(ldt2), pk, kscale);
#pragma unroll
    for (int j = 0; j < 57; ++j) asm volatile("" : "+v"(pk[j]));
    STAMP(5);
    if (regular) {
      if (STLATE == 2) { WAIT_VM(8); if (lane == 0) __hip_atomic_fetch_add((LAS u32*)(size_t)(lds0 + CT_FS), 1u, RLX_WG); }
      stage(2);
#pragma unroll 1
      for (int s = 0; s < 7; ++s) {
        WAIT_VM(8);
        rawbar();
        stage(s + 3);
        if (!SCONS && s == 3 && tid == 256) put_flag(ws + WS_FLAGS + (n80 * 32 + m64) * 8, tag);
      }
      if (side) { side_load(); WAIT_VM(13); } else WAIT_VM(8);
      rawbar();
      if (side) WAIT_VM(5); else WAIT_VM(0);
      rawbar();
      if (side) { WAIT_VM(0); side_read(); side_store(); }
    }
    STAMP(4);
    __builtin_amdgcn_s_setprio(KTPRIO);
    {
      const char* fp = lane < 32 ? ws + WS_FLAGS + (xcd * 32 + lane) * 8 : ws + WS_FLAGK + (xcd * 4 + (r >> 3)) * 8;
      const u64 f0 = __hip_atomic_load((const GAS u64*)fp, RLX_AGENT);
      ktable<false>(chb + half * SCR_W, chb + (half ? CH_KC1 : CH_KC0), pk, kscale, lane);
      if (regular && side) {
        WAIT_VM(0);
        if (lds_arrive(lds0 + CT_S, lane) == 1 && lane == 0) put_flag(ws + WS_FLAGW + (n80 * 32 + m64) * 8, tag);
      }
      if (!__all(f0 == tag)) {
        if (!regular) __builtin_amdgcn_s_sleep(HSLEEP);
        poll_flags<16>(fp, tag);
      }
      __builtin_amdgcn_fence(__ATOMIC_ACQUIRE, "wavefront");
      u32x4 ud[3], kx;
      u32 un[3];
      const u32 uo = WS_UT + (u32)(ch * (L * 2) + half * 2048);
#pragma unroll
      for (int i = 0; i < 2; ++i) {
        ud[i] = __builtin_amdgcn_raw_buffer_load_b128(wsr, lane * 16 + i * 1024, uo, 16);
        un[i] = __builtin_amdgcn_raw_buffer_load_b32(wsr, lane * 16 + i * 1024 + 16, uo, 16);
      }
      const u32 xo = WS_UT + (u32)((80 * xcd + 64 + (r >> 1)) * (L * 2) + qw * 1024);
      ud[2] = __builtin_amdgcn_raw_buffer_load_b128(wsr, lane * 16, xo, 16);
      un[2] = __builtin_amdgcn_raw_buffer_load_b32(wsr, lane * 16 + 16, xo, 16);
      kx = __builtin_amdgcn_raw_buffer_load_b128(wsr, lane * 16, WS_KX + (u32)((xcd * 16 + (r >> 1)) * 4096 + qw * 1024), 16);
      STAMP(1);
      lds_post(lds0 + CT_PAIR + (qw >> 1) * 4, lane);
      lds_wait(lds0 + CT_PAIR + (qw >> 1) * 4, 2);
      u32x4 c0, c1;
      expand(chb, half, lane, c0, c1);
      ST(u32x4, chb + CH_K + (64 * half + lane) * 16) = c0;
      ST(u32x4, chb + CH_K + (128 + 64 * half + lane) * 16) = c1;
      auto put2 = [&](u32 a, u32x4 d, u32 n) {
        ST(u32x4, a) = d;
        ST(u32x4, a + UCP) = (u32x4){__builtin_amdgcn_alignbit(d[1], d[0], 16), __builtin_amdgcn_alignbit(d[2], d[1], 16),
                                     __builtin_amdgcn_alignbit(d[3], d[2], 16), __builtin_amdgcn_alignbit(n, d[3], 16)};
      };
      auto edges = [&](u32 blk, u32x4 d) {
        const u32x4 z = {0, 0, 0, 0};
        if (lane < 2) put2(blk + CH_U + 16 - lane * 16, z, lane ? 0 : d[0]);
        else if (lane < 32) ST(u32x4, blk + CH_K + (254 + lane) * 16) = z;
      };
#pragma unroll
      for (int i = 0; i < 2; ++i) put2(chb + CH_U + 32 + half * 2048 + i * 1024 + lane * 16, ud[i], un[i]);
      if (half == 0) edges(chb, ud[0]);
      lds_wait(lds0 + CT_R, 4u);
      put2(lds0 + REG_B + CH_U + 32 + qw * 1024 + lane * 16, ud[2], un[2]);
      ST(u32x4, lds0 + REG_B + CH_K + qw * 1024 + lane * 16) = kx;
      if (qw == 0) edges(lds0 + REG_B, ud[2]);
    }
    lds_post(lds0 + CT_Q, lane);
    STAMP(8);
    __builtin_amdgcn_s_setprio(G2PRIO);
    if (regular) lds_wait(lds0 + CT_R, 4u);
    constexpr int NS = 14, DP = G2DP;
    const int first = qw >> 1;
    const int k0 = (qw < 2 ? 14 * qw : 28 + 13 * (qw - 2)) - first;
    const u32 hb = WS_HT + (u32)((m64 * 54 + k0) * 4096), wb = WS_W2T + (u32)((n80 * 54 + k0) * 5120);
    const u32 lo = lane * 16;
    u32x4 fr[DP][9];
    auto load1 = [&](int s, int i) {
      const int j = s;
      fr[s % DP][i] = i < 5 ? __builtin_amdgcn_raw_buffer_load_b128(wsr, lo + (i & 3) * 1024, wb + j * 5120 + (i >> 2) * 4096, 16)
                            : __builtin_amdgcn_raw_buffer_load_b128(wsr, lo + (i - 5) * 1024, hb + j * 4096, 16);
    };
    STAMP(6);
    if (qw == 0) {
      if (regular) lds_wait(lds0 + CT_H, 4u);
      poll_flags<HPS>(lane < 14 ? hfl + lane * 8 : ws + WS_FLAGW + (n80 * 32 + (lane >= 32 && lane < 59 ? lane - 32 : 0)) * 8, tag);
      if (lane == 0) __hip_atomic_store((LAS u32*)(size_t)(lds0 + CT_FH), 1u, RLX_WG);
    } else {
      lds_wait(lds0 + CT_FH, 1u);
    }
    STAMP(11);
#pragma unroll
    for (int s = 0; s < DP - 1; ++s)
      if (s >= first) {
#pragma unroll
        for (int i = 0; i < 9; ++i) load1(s, i);
      }
    f32x4 acc[5][4];
#pragma unroll
    for (int a = 0; a < 5; ++a)
#pragma unroll
      for (int b = 0; b < 4; ++b) acc[a][b] = (f32x4){0.f, 0.f, 0.f, 0.f};
    SB();
#pragma unroll
    for (int s = 0; s < NS; ++s) {
      if (s == YGATE && lane == 0) __hip_atomic_fetch_add((LAS u32*)(size_t)(lds0 + CT_FW), 1u, RLX_WG);
      if (s >= (STAG ? 3 : 1) || s >= first) {
#pragma unroll
        for (int a = 0; a < 5; ++a) {
          if (s + DP - 1 < NS) { load1(s + DP - 1, 2 * a); if (a < 4) load1(s + DP - 1, 2 * a + 1); }
#pragma unroll
          for (int b = 0; b < 4; ++b)
            acc[a][b] = mma(__builtin_bit_cast(bf16x8, fr[s % DP][a]), __builtin_bit_cast(bf16x8, fr[s % DP][5 + b]), acc[a][b]);
          SB();
        }
      } else {
        if (s + DP - 1 >= first) {
#pragma unroll
          for (int i = 0; i < 9; ++i) load1(s + DP - 1, i);
        }
        SB();
      }
    }
    STAMP(12);
    lds_wait(lds0 + CT_Y, 4u);
    const u32 sl = lds0 + REG_B + qw * SLAB + l15 * 336 + q4 * 16;
#pragma unroll
    for (int a = 0; a < 5; ++a)
#pragma unroll
      for (int b = 0; b < 4; ++b) ST(f32x4, sl + b * 16 * 336 + a * 64) = acc[a][b];
  } else {
    const bool lastn = ni == 13;
    u32x4 uu[3], dd[3];
    u32 b2v;
    auto ud_load = [&]() {
#pragma unroll
      for (int i = 0; i < 3; ++i) {
        const int c = 4 * i + q4 < 10 ? 4 * i + q4 : 9;
        uu[i] = *(const u32x4*)(P.u + (size_t)(64 * m64 + 16 * qw + l15) * D + 80 * n80 + 8 * c);
        dd[i] = *(const u32x4*)(P.dsk + 80 * n80 + 8 * c);
      }
      b2v = *(const u32*)(P.b2 + 80 * n80 + (tid < 80 ? tid & ~1 : 0));
    };
    auto ud_fence = [&]() { asm volatile("" : "+v"(uu[0]), "+v"(uu[1]), "+v"(uu[2]), "+v"(dd[0]), "+v"(dd[1]), "+v"(dd[2]), "+v"(b2v)); };
    if (!regular) { ud_load(); ud_fence(); }
    if (regular) {
      if (side) side_load();
      if (tid < 16) ST(u32, lds0 + LDS_CTL + tid * 4) = 0;
      bar();
      asm volatile("s_load_dwordx2 %0, %1, 0x0\n\ts_waitcnt lgkmcnt(0)" : "=&s"(tag) : "s"(ws) : "memory");
      tag += 1;
      WAIT_VM(0);
      STAMP(5);
      side_read();
      STAMP(6);
      __builtin_amdgcn_s_setprio(2);
      const int wf = qw & 1, wt = qw >> 1;
      u32 xa[2], wa[4][2];
      xa[0] = lds0 + 65536 + (u32)((64 * wt + l15) * 128 + ((q4 ^ (l15 >> 1)) * 16)); xa[1] = xa[0] ^ 64;
      f32x4 acc[4][4];
      if (!(lastn && wf)) {
#pragma unroll
        for (int t = 0; t < 2; ++t) {
          const int qq = l15 >> 2, p = lane & 3;
          const int row = 8 * q4 + 4 * t + qq, c = 8 * wf + (p >> 1);
          const u32 a0 = lds0 + 65536 + 16384u + 256u * row + 16u * (c ^ ((qq << 2) | ((2 * q4 + t) & 3))) + 8u * (p & 1);
#pragma unroll
          for (int ft = 0; ft < 4; ++ft) wa[ft][t] = a0 ^ (ft << 5);
        }
        bf16x8 A[2][4], B[2][4];
        auto rdf = [&](int set, int i, u32 so, int kk) {
          A[set][i] = mk8(tr_read(wa[i][0] + so + kk * 8192), tr_read(wa[i][1] + so + kk * 8192));
          B[set][i] = LD(bf16x8, xa[kk] + so + i * 2048);
        };
        auto hstep = [&](int set, u32 so, int kk) {
          constexpr int ord[16][2] = {{0, 0}, {0, 1}, {1, 0}, {1, 1}, {0, 2}, {1, 2}, {2, 0}, {2, 1},
                                      {2, 2}, {0, 3}, {1, 3}, {2, 3}, {3, 0}, {3, 1}, {3, 2}, {3, 3}};
#pragma unroll
          for (int g = 0; g < 4; ++g) {
            rdf(set ^ 1, g, so, kk);
#pragma unroll
            for (int j = 0; j < 4; ++j) {
              const int a = ord[4 * g + j][0], b = ord[4 * g + j][1];
              acc[a][b] = mma(A[set][a], B[set][b], acc[a][b]);
            }
#pragma unroll
            for (int j = 0; j < 3; ++j) { __builtin_amdgcn_sched_group_barrier(0x8, 1, 0); __builtin_amdgcn_sched_group_barrier(0x100, 1, 0); }
            __builtin_amdgcn_sched_group_barrier(0x8, 1, 0);
            SB();
          }
        };
        bar();
        STAMP(1);
#pragma unroll
        for (int i = 0; i < 4; ++i) rdf(0, i, 0, 0);
        if (STLATE == 1 && side) side_store();
#pragma unroll
        for (int ft = 0; ft < 4; ++ft) {
          const u32x2 bi = LD(u32x2, lds0 + LDS_B1 + (wf * 64 + ft * 16 + 4 * q4) * 2);
#pragma unroll
          for (int tt = 0; tt < 4; ++tt) acc[ft][tt] = (f32x4){bflo(bi.x), bfhi(bi.x), bflo(bi.y), bfhi(bi.y)};
        }
        auto flip = [&]() {
          xa[0] ^= 65536; xa[1] ^= 65536;
#pragma unroll
          for (int ft = 0; ft < 4; ++ft) { wa[ft][0] ^= 65536; wa[ft][1] ^= 65536; }
          SB();
        };
#pragma unroll 1
        for (int p = 0; p < 4; ++p) {
          hstep(0, 0, 1); hstep(1, 32768, 0);
          if (p == 1) WAIT_VM(0);
          rawbar();
          if (SCONS && p == 1 && tid == 0) put_flag(ws + WS_FLAGS + (n80 * 32 + m64) * 8, tag);
          SB();
          hstep(0, 32768, 1);
          flip();
          hstep(1, 0, 0);
          rawbar(); SB();
        }
        ud_load(); SB();
        hstep(0, 0, 1); hstep(1, 32768, 0); hstep(0, 32768, 1); hstep(1, 0, 0);
        ud_fence();
        STAMP(2);
        const u32 ho = WS_HT + (u32)(((2 * mi + wt) * 54 + 4 * ni + 2 * wf) * 4096);
#pragma unroll
        for (int kh = 0; kh < 2; ++kh)
#pragma unroll
          for (int tt = 0; tt < 4; ++tt) {
            u32x4 o;
#pragma unroll
            for (int h = 0; h < 2; ++h) {
              f32x4 v = __builtin_bit_cast(f32x4, __builtin_elementwise_max(__builtin_bit_cast(i32x4, acc[2 * kh + h][tt]), (i32x4){0, 0, 0, 0}));
              v = v * v;
              o[2 * h] = pk_bf16(v[0], v[1]); o[2 * h + 1] = pk_bf16(v[2], v[3]);
            }
            __builtin_amdgcn_raw_buffer_store_b128(o, wsr, ho + kh * 4096 + lane * 16 + tt * 1024, 0, 16);
          }
      } else {
#pragma unroll
        for (int tt = 0; tt < 4; ++tt) acc[0][tt] = (f32x4){0.f, 0.f, 0.f, 0.f};
#pragma unroll 1
        for (int s = 0; s < 10; ++s) {
          if (s == 3) WAIT_VM(0);
          if (s < 9) bar();
          if (STLATE == 1 && s == 0 && side) side_store();
          const int k = s + rot >= 10 ? s + rot - 10 : s + rot;
#pragma unroll
          for (int kk = 0; kk < 2; ++kk) {
            const bf16x8 Ag = LD(bf16x8, lds0 + LDS_GW + k * 128 + kk * 64 + q4 * 16);
#pragma unroll
            for (int tt = 0; tt < 4; ++tt) acc[0][tt] = mma(Ag, LD(bf16x8, (xa[kk] ^ (((s + 2) & 3) * 32768) ^ 65536) + tt * 2048), acc[0][tt]);
          }
        }
        ud_load(); ud_fence();
        float pre = q4 == 0 ? acc[0][0][0] : q4 == 1 ? acc[0][1][0] : q4 == 2 ? acc[0][2][0] : acc[0][3][0];
        pre = rbf(pre);
        const float g = rbf(1.f / (1.f + __expf(-pre)));
        __builtin_amdgcn_raw_buffer_store_b32(__float_as_uint(g), wsr, WS_GATE + (u32)(mi * 128 + 64 * wt + lane) * 4, 0, 16);
      }
      if (lane == 0) __hip_atomic_fetch_add((LAS u32*)(size_t)(lds0 + CT_R), 1u, RLX_WG);
      STAMP(3);
    }
    __builtin_amdgcn_s_setprio(CVPRIO);
    {
      if (regular) {
        lds_wait(lds0 + CT_R, 4u);
      } else {
        lds_wait(lds0 + CT_PAIR, 2); lds_wait(lds0 + CT_PAIR + 4, 2);
      }
#pragma unroll
      for (int i = 0; i < 3; ++i) {
        const int c = 4 * i + q4 < 10 ? 4 * i + q4 : 9;
        u32x4 v;
#pragma unroll
        for (int e = 0; e < 4; ++e) v[e] = pk_bf16(bflo(uu[i][e]) * bflo(dd[i][e]), bfhi(uu[i][e]) * bfhi(dd[i][e]));
        ST(u32x4, lds0 + LDS_GA + (16 * qw + l15) * 160 + c * 16) = v;
      }
      if (tid < 80) ST(float, lds0 + LDS_B2 + tid * 4) = (tid & 1) ? bfhi(b2v) : bflo(b2v);
    }
    if (regular) {
      WAIT_VM(0);
      if (lds_arrive(lds0 + CT_H, lane) == 3 && lane < 16) put_flag(ws + WS_INH + ((mi * 16 + lane) * 16 + ni) * 8, tag);
      STAMP(4);
    }
    lds_wait(lds0 + CT_Q, 4u);
    STAMP(8);
    {
      f32x4 acc[8], acx[8];
#pragma unroll
      for (int j = 0; j < 8; ++j) { acc[j] = (f32x4){0.f, 0.f, 0.f, 0.f}; acx[j] = acc[j]; }
      const int z = l15 + 8 * q4 + 1;
      const u32 oa = CH_U + (z & 1) * UCP + (z & ~1) * 2, ob = CH_K + (2032 - 16 * l15 + 8 * q4) * 2 - 512 * 7;
      const u32 b0 = chb + half * 256, b1 = lds0 + REG_B + (4 * (r & 1) + qw) * 64;
      u32 pa = b0 + oa, pa8 = pa + 8, pb = b0 + ob, xa = b1 + oa, xa8 = xa + 8, xb = b1 + ob;
      asm volatile("" : "+v"(pa8), "+v"(xa8));
      ConvFrags f;
#pragma unroll
      for (int k = 0; k < 8; ++k) { f.A[0][k] = mk8(ld2(pa + 512 * k), ld2(pa8 + 512 * k)); f.B[0][7 - k] = LD(bf16x8, pb + 512 * k); }
      SB();
      conv_step<true>(f, 0, pa + 64, pa8 + 64, pb + 64, acc);
      conv_step<true>(f, 1, pa + 128, pa8 + 128, pb + 128, acc);
      conv_step<true>(f, 0, pa + 192, pa8 + 192, pb + 192, acc);
      conv_step<true>(f, 1, xa, xa8, xb, acc);
      conv_step<false>(f, 0, 0, 0, 0, acx);
      STAMP(9);
      if (half) {
#pragma unroll
        for (int j = 0; j < 4; ++j) { const f32x4 t = acc[j]; acc[j] = acc[4 + j]; acc[4 + j] = t; asm volatile("" : "+v"(acc[j]), "+v"(acc[4 + j])); }
      }
      const u32 x1 = lds0 + LDS_X1 + lane * 16, x2 = lds0 + LDS_X2 + lane * 16;
#pragma unroll
      for (int j = 0; j < 4; ++j) ST(f32x4, x1 + qw * 4096 + j * 1024) = acc[4 + j];
#pragma unroll
      for (int j = 0; j < 8; ++j) ST(f32x4, x2 + qw * 8192 + j * 1024) = acx[j];
      lds_post(lds0 + CT_Q, lane);
      lds_wait(lds0 + CT_Q, 8u);
      auto y_store = [&](f32x4 v, f32x4 w, u32 o) {
        const auto a = __builtin_amdgcn_permlane16_swap(pk_bf16(v[0], v[1]), pk_bf16(w[0], w[1]), false, false);
        const auto b = __builtin_amdgcn_permlane16_swap(pk_bf16(v[2], v[3]), pk_bf16(w[2], w[3]), false, false);
        __builtin_amdgcn_raw_buffer_store_b128((u32x4){a[0], b[0], a[1], b[1]}, wsr, WS_YT + o + (q4 & 1) * 512 + (16 * l15 + 8 * (q4 >> 1)) * 2, 0, 16);
      };
#pragma unroll
      for (int j = 0; j < 4; j += 2)
        y_store(acc[j] + LD(f32x4, x1 + (qw ^ 1) * 4096 + j * 1024), acc[j + 1] + LD(f32x4, x1 + (qw ^ 1) * 4096 + j * 1024 + 1024),
                (u32)(ch * (L * 2) + (4 * half + j) * 512));
      {
        f32x4 v[2];
#pragma unroll
        for (int j = 0; j < 2; ++j) {
          const u32 o = x2 + (2 * qw + j) * 1024;
          v[j] = (LD(f32x4, o) + LD(f32x4, o + 8192)) + (LD(f32x4, o + 16384) + LD(f32x4, o + 24576));
        }
        y_store(v[0], v[1], (u32)((((r & 1) ? D + 16 * xcd : 80 * xcd + 64) + (r >> 1)) * (L * 2) + 2 * qw * 512));
      }
      WAIT_VM(0);
      if (lds_arrive(lds0 + CT_Y, lane) == 3u) {
        if (lane == 32) put_flag(ws + WS_FLAGY + (xcd * 32 + r) * 8, tag);
        if (INY && lane < 32) put_flag(ws + WS_INY + ((lane * 8 + xcd) * 32 + r) * 8, tag);
      }
      STAMP(10);
    }
    if (qw == 0) {
      lds_wait(lds0 + CT_Y, 4u);
      lds_wait(lds0 + CT_FW, 4u);
      poll_flags<YPS>(lane < 32 ? yfl + lane * 8 : hfl + 13 * 8, tag);
      if (lane == 0) __hip_atomic_store((LAS u32*)(size_t)(lds0 + CT_FY), 1u, RLX_WG);
    } else {
      lds_wait(lds0 + CT_FY, 1u);
    }
    STAMP(11);
    const float g = __uint_as_float(__builtin_amdgcn_raw_buffer_load_b32(wsr, (16 * qw + l15) * 4, WS_GATE + (u32)(64 * m64) * 4, 16));
    u32x4 yy[3];
#pragma unroll
    for (int i = 0; i < 3; ++i) {
      const int row = (tid + 256 * i) >> 3;
      yy[i] = __builtin_amdgcn_raw_buffer_load_b128(wsr, (row < 80 ? 80 * n80 + row : D - 80 + 16 * n80 + row) * (L * 2) + (tid & 7) * 16,
                                                    WS_YT + (u32)(64 * m64 * 2), 16);
    }
    if (q4 == 0) ST(float, lds0 + LDS_OMG + (16 * qw + l15) * 4) = g;
#pragma unroll
    for (int i = 0; i < 3; ++i) ST(u32x4, lds0 + LDS_YT + ((tid + 256 * i) >> 3) * 144 + (tid & 7) * 16) = yy[i];
    if (bid == 0 && wave == 3) {
#pragma unroll 1
      for (int i = 0; i < 4; ++i) poll_flags<16>(ws + WS_FLAGY + (i * 64 + lane) * 8, tag);
      if (lane == 0) put_flag(ws, tag);
    }
  }
  STAMP(13);
  bar();
  STAMP(14);
  auto fin = [&](int unit, bool last) {
    const int tt = last ? 2 * (unit - 8) + (q4 >> 1) : unit & 3, c8 = last ? 8 + (q4 & 1) : 4 * (unit >> 2) + q4, t = 16 * tt + l15;
    const u32 yp = lds0 + LDS_YT + (8 * c8 + (l15 >> 2)) * 144 + tt * 32 + 8 * (lane & 3);
    const u32x2 ya = tr_read(yp), yb = tr_read(yp + 4 * 144);
    u32x2 za = {0, 0}, zb = {0, 0};
    if (last) { za = tr_read(yp + 16 * 144); zb = tr_read(yp + 20 * 144); }
    f32x4 s0 = LD(f32x4, lds0 + LDS_B2 + c8 * 32), s1 = LD(f32x4, lds0 + LDS_B2 + c8 * 32 + 16);
#pragma unroll
    for (int v = 0; v < 4; ++v) {
      const u32 p = lds0 + REG_B + v * SLAB + t * 336 + c8 * 32;
      s0 += LD(f32x4, p); s1 += LD(f32x4, p + 16);
    }
    const u32x4 ud = LD(u32x4, lds0 + LDS_GA + t * 160 + c8 * 16);
    const float g = LD(float, lds0 + LDS_OMG + t * 4), om = rbf(1.f - g);
    u32x4 o;
#pragma unroll
    for (int e = 0; e < 4; ++e) {
      const u32 y = e < 2 ? ya[e] : yb[e - 2], y2 = e < 2 ? za[e] : zb[e - 2];
      float a0 = bflo(y) + bflo(ud[e]), a1 = bfhi(y) + bfhi(ud[e]);
      if (last) { a0 += bflo(y2); a1 += bfhi(y2); }
      const float x0 = e < 2 ? s0[2 * e] : s1[2 * e - 4], x1 = e < 2 ? s0[2 * e + 1] : s1[2 * e - 3];
      o[e] = pk_bf16(g * a0 + om * x0, g * a1 + om * x1);
    }
    *(u32x4*)(P.out + (size_t)(64 * m64 + t) * D + 80 * n80 + 8 * c8) = o;
  };
  if (wave < 2) { fin(wave, false); fin(wave + 8, true); } else fin(wave, false);
  STAMP(15);
}

extern "C" void kernel_launch(void* const* d_in, const int* in_sizes, int n_in, void* d_out, int out_size,
                              void* d_ws, size_t ws_size, hipStream_t stream) {
  Params P;
  P.u = (const u16*)d_in[0]; P.lar = (const u16*)d_in[1]; P.aim = (const u16*)d_in[2]; P.cre = (const u16*)d_in[3];
  P.cim = (const u16*)d_in[4]; P.ldt = (const u16*)d_in[5]; P.dsk = (const u16*)d_in[6]; P.w1 = (const u16*)d_in[8];
  P.b1 = (const u16*)d_in[9]; P.w2 = (const u16*)d_in[10]; P.b2 = (const u16*)d_in[11]; P.gw = (const u16*)d_in[12];
  P.out = (u16*)d_out; P.ws = (char*)d_ws;
  (void)hipFuncSetAttribute((const void*)fused, hipFuncAttributeMaxDynamicSharedMemorySize, LDS_TOTAL);
  fused<<<256, 512, LDS_TOTAL, stream>>>(P);
}
```
